```python
import torch

def _softmax(x, axis=-1):
    return torch.softmax(x, dim=axis)

def _gelu(x):
    return torch.nn.functional.gelu(x, approximate='tanh')

def _silu(x):
    return torch.nn.functional.silu(x)

def _sigmoid(x):
    return torch.sigmoid(x)

def _layernorm(x, axis=-1, eps=1e-05):
    m = x.mean(dim=axis, keepdim=True)
    v = x.var(dim=axis, keepdim=True, unbiased=False)
    return (x - m) / torch.sqrt(v + eps)

def _scatter_add(tgt, idx, src, axis=0):
    return tgt.clone().index_add_(axis, idx, src)

def _segment_sum(src, seg, n):
    out = torch.zeros((n,) + src.shape[1:], dtype=src.dtype)
    return out.index_add_(0, seg, src)

def _pad(x, pw):
    flat = tuple((v for pair in reversed(pw) for v in pair))
    return torch.nn.functional.pad(x, flat)
kernel_name = 'adaLN_cond__ssm_conv_h3__glu_gated_swiglu__prompt'

def setup_inputs():

    _w = lambda a: (lambda t: t.to(torch.bfloat16) if t.is_floating_point() else t)(torch.as_tensor(a))

    def _A_setup():
        B, S, D = (1, 2048, 1024)
        ks = 4
        return {'q': _w((lambda *s: torch.randn(*s, dtype=torch.bfloat16) * 0.05)(B, S, D)), 'k': _w((lambda *s: torch.randn(*s, dtype=torch.bfloat16) * 0.8)(B, S, D)), 'v': _w((lambda *s: torch.randn(*s, dtype=torch.bfloat16) * 0.8)(B, S, D)), 'shift_k': _w((lambda *s: torch.randn(*s, dtype=torch.bfloat16) * 0.8)(D, ks) * 0.5), 'ssm_k': _w((lambda *s: torch.randn(*s, dtype=torch.bfloat16) * 0.8)(D, S) * (1.0 / S ** 0.5)), 'ssm_D': _w((lambda *s: torch.randn(*s, dtype=torch.bfloat16) * 0.8)(D))}
    _A_in = _A_setup()

    def _B_setup():
        B, S, D, F = (1, 2048, 1024, 3584)
        return {'x': _w((lambda *s: torch.randn(*s, dtype=torch.bfloat16) * 0.05)(B, S, D)), 'w1': _w((lambda *s: torch.randn(*s, dtype=torch.bfloat16) * 0.8)(D, F) * (2.0 / D) ** 0.5), 'w3': _w((lambda *s: torch.randn(*s, dtype=torch.bfloat16) * 0.8)(D, F) * (2.0 / D) ** 0.5), 'w2': _w((lambda *s: torch.randn(*s, dtype=torch.bfloat16) * 0.8)(F, D) * (2.0 / F) ** 0.5)}
    _B_in = _B_setup()
    return {'A_q': _A_in['q'], 'A_k': _A_in['k'], 'A_v': _A_in['v'], 'A_shift_k': _A_in['shift_k'], 'A_ssm_k': _A_in['ssm_k'], 'A_ssm_D': _A_in['ssm_D'], 'B_x': _B_in['x'], 'B_w1': _B_in['w1'], 'B_w3': _B_in['w3'], 'B_w2': _B_in['w2'], 'c': _w((lambda *s: torch.randn(*s, dtype=torch.bfloat16) * 0.2)(1, 1024) * 8.0), 'w_mod': _w((lambda *s: torch.randn(*s, dtype=torch.bfloat16) * 0.2)(1024, 6 * 1024) * 1024 ** (-0.5) * 8.0)}

def reference(A_q, A_k, A_v, A_shift_k, A_ssm_k, A_ssm_D, B_x, B_w1, B_w3, B_w2, c, w_mod):

    def _A_ref(q, k, v, shift_k, ssm_k, ssm_D):
        B, S, D = q.shape
        ks = shift_k.shape[-1]
        k_pad = _pad(k, ((0, 0), (ks - 1, 0), (0, 0)))
        k_shift = torch.zeros_like(k)
        for t in range(ks):
            k_shift = k_shift + k_pad[:, t:t + S, :] * shift_k[:, ks - 1 - t][None, None, :]
        kv = k_shift * v
        L = 2 * S
        kv_pad = _pad(kv, ((0, 0), (0, S), (0, 0)))
        kf_pad = _pad(ssm_k, ((0, 0), (0, S)))
        kv_f = (lambda x, axes=(-2, -1): torch.fft.rfft2(x.float(), dim=axes))(kv_pad[:, None, :, :], axes=(1, 2))
        kf_f = (lambda x, axes=(-2, -1): torch.fft.rfft2(x.float(), dim=axes))(kf_pad[:, None, :], axes=(1, 2))
        kf_f = (lambda x, *d: x.permute(*d))(kf_f, 1, 2, 0)
        y_f = kv_f * kf_f[None, :, :, :]
        y = (lambda x, s, axes=(-2, -1): torch.fft.irfft2(x, s=s, dim=axes).to(torch.bfloat16))(y_f, (1, L), axes=(1, 2))[:, 0, :S, :]
        y = y + kv * ssm_D[None, None, :]
        return q * y

    def _B_ref(x, w1, w3, w2):
        gate = torch.einsum('bsd,df->bsf', x, w1)
        up = torch.einsum('bsd,df->bsf', x, w3)
        h = _silu(gate) * up
        out = torch.einsum('bsf,fd->bsd', h, w2)
        return out
    x = A_q
    mod = (_silu(c) @ w_mod).reshape(c.shape[0], 6, 1, -1)
    sa, ca, ga, sb, cb, gb = (mod[:, 0].clone(), mod[:, 1].clone(), mod[:, 2].clone(), mod[:, 3].clone(), mod[:, 4].clone(), mod[:, 5].clone())
    h = _layernorm(x, axis=-1) * (1 + ca) + sa
    x = x + ga * _A_ref(h, A_k, A_v, A_shift_k, A_ssm_k, A_ssm_D)
    h = _layernorm(x, axis=-1) * (1 + cb) + sb
    x = x + gb * _B_ref(h, B_w1, B_w3, B_w2)
    return x
```

```cpp
#include <hip/hip_runtime.h>
#include <stdint.h>

typedef unsigned short u16;
typedef unsigned int u32;
typedef short s16x4 __attribute__((ext_vector_type(4)));
typedef short s16x8 __attribute__((ext_vector_type(8)));
typedef __bf16 bf16x2 __attribute__((ext_vector_type(2)));
typedef __bf16 bf16x8 __attribute__((ext_vector_type(8)));
typedef float f32x2 __attribute__((ext_vector_type(2)));
typedef float f32x4 __attribute__((ext_vector_type(4)));
typedef u32 u32x2 __attribute__((ext_vector_type(2)));
typedef u32 u32x4 __attribute__((ext_vector_type(4)));
#define LDSP __attribute__((address_space(3)))

constexpr int S_ = 2048, D_ = 1024, F_ = 3584;

constexpr size_t WS_MOD = 0;
constexpr size_t WS_YP = 1u << 20;
constexpr size_t WS_X1 = WS_YP + (4u << 20);
constexpr size_t WS_H2 = WS_X1 + (4u << 20);
constexpr size_t WS_HH = WS_H2 + (4u << 20);

__device__ __forceinline__ float bf_lo(u32 v) { return __uint_as_float(v << 16); }
__device__ __forceinline__ float bf_hi(u32 v) { return __uint_as_float(v & 0xffff0000u); }
__device__ __forceinline__ u32 pk_bf(float lo, float hi) {
  f32x2 f = {lo, hi};
  bf16x2 b = __builtin_convertvector(f, bf16x2);
  return __builtin_bit_cast(u32, b);
}
__device__ __forceinline__ float rbf(float x) { return bf_lo(pk_bf(x, x)); }
__device__ __forceinline__ float silu_f(float x) {
  return x * __builtin_amdgcn_rcpf(1.0f + __builtin_amdgcn_exp2f(x * -1.44269504088896341f));
}
__device__ __forceinline__ f32x4 mfma16(bf16x8 a, bf16x8 b, f32x4 c) {
  return __builtin_amdgcn_mfma_f32_16x16x32_bf16(a, b, c, 0, 0, 0);
}
__device__ __forceinline__ void glds16(const void* sbase, u32 voff, u32 ldsoff) {
  asm volatile("s_mov_b32 m0, %2\n\ts_nop 0\n\tglobal_load_lds_dwordx4 %0, %1"
               :: "v"(voff), "s"(sbase), "s"(ldsoff) : "memory");
}
__device__ __forceinline__ void glds16_48(const void* sbase, u32 voff, u32 ldsoff) {
  asm volatile("s_mov_b32 m0, %2\n\ts_mov_b32 exec_hi, 0xffff\n\tglobal_load_lds_dwordx4 %0, %1\n\ts_mov_b32 exec_hi, -1"
               :: "v"(voff), "s"(sbase), "s"(ldsoff) : "memory");
}
#define SCHED0() __builtin_amdgcn_sched_barrier(0)
#define WAIT_VM(n) do { SCHED0(); asm volatile("s_waitcnt vmcnt(%0)" :: "n"(n) : "memory"); } while (0)
#define BARRIER() do { asm volatile("s_waitcnt lgkmcnt(0)\n\ts_barrier" ::: "memory"); SCHED0(); } while (0)

__device__ __forceinline__ bf16x8 lds_b128(u32 off) {
  return *(const LDSP bf16x8*)(off);
}
__device__ __forceinline__ bf16x8 lds_tr2(u32 off0, u32 off1) {
  s16x4 a = __builtin_amdgcn_ds_read_tr16_b64_v4i16((LDSP s16x4*)(off0));
  s16x4 b = __builtin_amdgcn_ds_read_tr16_b64_v4i16((LDSP s16x4*)(off1));
  s16x8 ab = __builtin_shufflevector(a, b, 0, 1, 2, 3, 4, 5, 6, 7);
  return __builtin_bit_cast(bf16x8, ab);
}

constexpr int C_KPAD = 448;
constexpr int C_KE = C_KPAD;
constexpr int C_KO = 4576 + C_KPAD;
constexpr int C_R1 = 9152;
constexpr int C_R0 = C_R1 + 4224;
constexpr int C_CH = 9152 + 8384;
constexpr int C_GP = 4 * C_CH;
constexpr int C_LDS = C_GP + 2048;

__global__ __launch_bounds__(512, 2) void k0_conv(
    const u16* __restrict__ Kin, const u16* __restrict__ Vin, const u16* __restrict__ shk,
    const u16* __restrict__ ssmk, const u16* __restrict__ ssmD, const u16* __restrict__ cvec,
    const u16* __restrict__ wmod, u16* __restrict__ modout, u16* __restrict__ yp) {
  extern __shared__ __attribute__((aligned(16))) char smem[];
  const int t = threadIdx.x, lane = t & 63;
  const int w = __builtin_amdgcn_readfirstlane(t >> 6);
  const int b = blockIdx.x;
  const int grp = (b & 7) * 32 + (b >> 3);
  const int d0 = grp * 4;
  const u32 lbase = (u32)(uintptr_t)(LDSP char*)smem;

  const u16* fk = ssmk + (size_t)d0 * S_;
  u32x4 fc[2]; u32 fp[2];
#pragma unroll
  for (int i = 0; i < 2; ++i) {
    int z = t + 512 * i;
    fc[i] = *(const u32x4*)(fk + z * 8);
    int zz = (z & 255) ? z : z + 1;
    fp[i] = *(const u32*)(fk + zz * 8 - 2);
  }
  u32x2 kr[4], vr[4], hk[3];
  const u16* kp = Kin + (size_t)(4 * t) * D_ + d0;
  const u16* vp = Vin + (size_t)(4 * t) * D_ + d0;
#pragma unroll
  for (int r = 0; r < 4; ++r) kr[r] = *(const u32x2*)(kp + r * D_);
#pragma unroll
  for (int r = 0; r < 4; ++r) vr[r] = *(const u32x2*)(vp + r * D_);
#pragma unroll
  for (int r = 0; r < 3; ++r) hk[r] = (u32x2){0u, 0u};
  if (lane == 0 && t != 0) {
#pragma unroll
    for (int r = 0; r < 3; ++r) hk[r] = *(const u32x2*)(kp + (r - 3) * D_);
  }
  u32x4 wq[16]; u32x4 cq[2];
  const int gq = lane & 3, rho = lane >> 2;
  if (w >= 4) {
    const int row0 = (w - 4) * 256 + rho * 16;
    cq[0] = *(const u32x4*)(cvec + row0);
    cq[1] = *(const u32x4*)(cvec + row0 + 8);
    const u16* wp = wmod + (size_t)row0 * 6144 + grp * 24 + (gq < 3 ? gq : 2) * 8;
#pragma unroll
    for (int i = 0; i < 16; ++i) wq[i] = *(const u32x4*)(wp + (size_t)i * 6144);
  }
  const u32x4 sk01 = *(const u32x4*)(shk + d0 * 4);
  const u32x4 sk23 = *(const u32x4*)(shk + d0 * 4 + 8);
  const u32x2 dD = *(const u32x2*)(ssmD + d0);

  if (t < 224) {
    int ch = t / 56, rem = t % 56, cp = rem / 28, i = rem % 28;
    *(LDSP u32x4*)(lbase + ch * C_CH + cp * 4576 + i * 16) = (u32x4){0u, 0u, 0u, 0u};
  } else if (t >= 256 && t < 256 + 64) {
    int i = t - 256, ch = i >> 4, cp = (i >> 3) & 1, j = i & 7;
    *(LDSP u32x2*)(lbase + ch * C_CH + (cp ? C_R0 : C_R1) + 4096 + j * 8) = (u32x2){0u, 0u};
  } else if (t >= 320 && t < 324) {
    *(LDSP u32*)(lbase + (t - 320) * C_CH + C_R0 + 4092) = 0u;
  }
#pragma unroll
  for (int i = 0; i < 2; ++i) {
    int z = t + 512 * i, ch = z >> 8, zc = z & 255;
    u32 prev = zc ? fp[i] : 0u;
    u32x4 e;
    e[0] = __builtin_amdgcn_alignbit(fc[i][0], prev, 16);
    e[1] = __builtin_amdgcn_alignbit(fc[i][1], fc[i][0], 16);
    e[2] = __builtin_amdgcn_alignbit(fc[i][2], fc[i][1], 16);
    e[3] = __builtin_amdgcn_alignbit(fc[i][3], fc[i][2], 16);
    *(LDSP u32x4*)(lbase + ch * C_CH + C_KE + zc * 16) = e;
    *(LDSP u32x4*)(lbase + ch * C_CH + C_KO + zc * 16) = fc[i];
  }
  u32 upk[4][2];
  {
#pragma unroll
    for (int r = 0; r < 3; ++r) {
      hk[r][0] = __builtin_amdgcn_update_dpp(hk[r][0], kr[r + 1][0], 0x138, 0xf, 0xf, false);
      hk[r][1] = __builtin_amdgcn_update_dpp(hk[r][1], kr[r + 1][1], 0x138, 0xf, 0xf, false);
    }
#pragma unroll
    for (int ch = 0; ch < 4; ++ch) {
      const u32x4 sk = ch < 2 ? sk01 : sk23;
      const u32 s0 = sk[(ch & 1) * 2], s1 = sk[(ch & 1) * 2 + 1];
      const float w0 = bf_lo(s0), w1 = bf_hi(s0), w2 = bf_lo(s1), w3 = bf_hi(s1);
      float kf[7];
#pragma unroll
      for (int r = 0; r < 7; ++r) {
        u32 dw = r < 3 ? hk[r][ch >> 1] : kr[r - 3][ch >> 1];
        kf[r] = (ch & 1) ? bf_hi(dw) : bf_lo(dw);
      }
      float u[4];
#pragma unroll
      for (int r = 0; r < 4; ++r) {
        float a = kf[r] * w3;
        a = fmaf(kf[r + 1], w2, a);
        a = fmaf(kf[r + 2], w1, a);
        a = fmaf(kf[r + 3], w0, a);
        u32 dv = vr[r][ch >> 1];
        u[r] = a * ((ch & 1) ? bf_hi(dv) : bf_lo(dv));
      }
      upk[ch][0] = pk_bf(u[3], u[2]);
      upk[ch][1] = pk_bf(u[1], u[0]);
      *(LDSP u32x2*)(lbase + ch * C_CH + C_R1 + (2044 - 4 * t) * 2) = (u32x2){upk[ch][0], upk[ch][1]};
      LDSP u32* r0 = (LDSP u32*)(lbase + ch * C_CH + C_R0 + (2042 - 4 * t) * 2);
      r0[0] = upk[ch][0];
      r0[1] = upk[ch][1];
    }
  }
  __syncthreads();

  if (w < 4) {
    const int g = lane >> 4, i16 = lane & 15;
    const u32 cb = lbase + w * C_CH;
    u32 la = cb + ((i16 & 1) ? C_KO : C_KE) + 64 * (i16 >> 1) + 16 * g;
    u32 lb = cb + ((i16 & 1) ? C_R1 : C_R0) + 4088 - 8 * (i16 >> 1) + 16 * g - 3584;
    f32x4 acc[8];
#pragma unroll
    for (int c = 0; c < 8; ++c) acc[c] = (f32x4){0.f, 0.f, 0.f, 0.f};
#pragma unroll 2
    for (int qp = 0; qp < 8; ++qp) {
      bf16x8 A[8], B[8];
#pragma unroll
      for (int e = 0; e < 8; ++e) {
        u32x2 lo = *(volatile const LDSP u32x2*)(lb + 512 * (7 - e));
        u32x2 hi = *(volatile const LDSP u32x2*)(lb + 512 * (7 - e) + 8);
        u32x4 v = {lo[0], lo[1], hi[0], hi[1]};
        B[e] = __builtin_bit_cast(bf16x8, v);
        A[e] = lds_b128(la + 512 * e);
      }
#pragma unroll
      for (int a = 0; a < 8; ++a)
#pragma unroll
        for (int e = 0; e < 8 - a; ++e) acc[a + e] = mfma16(A[a], B[e], acc[a + e]);
      la -= 64; lb -= 64;
    }
#pragma unroll
    for (int c = 0; c < 8; ++c) {
      u32 o = cb + (256 * c + 64 * g + 2 * i16) * 2;
      *(LDSP u32*)(o) = pk_bf(acc[c][0], acc[c][1]);
      *(LDSP u32*)(o + 64) = pk_bf(acc[c][2], acc[c][3]);
    }
  } else {
    u32 sc[8];
#pragma unroll
    for (int i = 0; i < 8; ++i) {
      u32 cv = cq[i >> 2][i & 3];
      sc[i] = pk_bf(silu_f(bf_lo(cv)), silu_f(bf_hi(cv)));
    }
    float ga[8];
#pragma unroll
    for (int j = 0; j < 8; ++j) ga[j] = 0.f;
#pragma unroll
    for (int i = 0; i < 8; ++i) {
#pragma unroll
      for (int j = 0; j < 4; ++j) {
        u32 a = wq[2 * i][j], bb = wq[2 * i + 1][j];
        u32 lo = __builtin_amdgcn_perm(bb, a, 0x05040100u);
        u32 hi = __builtin_amdgcn_perm(bb, a, 0x07060302u);
        ga[2 * j] = __builtin_amdgcn_fdot2_f32_bf16(__builtin_bit_cast(bf16x2, lo), __builtin_bit_cast(bf16x2, sc[i]), ga[2 * j], false);
        ga[2 * j + 1] = __builtin_amdgcn_fdot2_f32_bf16(__builtin_bit_cast(bf16x2, hi), __builtin_bit_cast(bf16x2, sc[i]), ga[2 * j + 1], false);
      }
    }
#pragma unroll
    for (int j = 0; j < 8; ++j) {
      ga[j] += __uint_as_float(__builtin_amdgcn_update_dpp(0u, __float_as_uint(ga[j]), 0x114, 0xf, 0xf, true));
      ga[j] += __uint_as_float(__builtin_amdgcn_update_dpp(0u, __float_as_uint(ga[j]), 0x118, 0xf, 0xf, true));
    }
    if ((lane & 15) >= 12) {
      u32 o = lbase + C_GP + (((w - 4) * 4 + (lane >> 4)) * 32 + gq * 8) * 4;
      *(LDSP f32x4*)(o) = (f32x4){ga[0], ga[1], ga[2], ga[3]};
      *(LDSP f32x4*)(o + 16) = (f32x4){ga[4], ga[5], ga[6], ga[7]};
    }
  }
  __syncthreads();

  {
    float yv[4][4];
#pragma unroll
    for (int ch = 0; ch < 4; ++ch) {
      u32x2 y = *(const LDSP u32x2*)(lbase + ch * C_CH + 8 * t);
      const float Dv = (ch & 1) ? bf_hi(dD[ch >> 1]) : bf_lo(dD[ch >> 1]);
      u32 p0 = pk_bf(bf_hi(upk[ch][1]) * Dv, bf_lo(upk[ch][1]) * Dv);
      u32 p1 = pk_bf(bf_hi(upk[ch][0]) * Dv, bf_lo(upk[ch][0]) * Dv);
      yv[0][ch] = bf_lo(y[0]) + bf_lo(p0);
      yv[1][ch] = bf_hi(y[0]) + bf_hi(p0);
      yv[2][ch] = bf_lo(y[1]) + bf_lo(p1);
      yv[3][ch] = bf_hi(y[1]) + bf_hi(p1);
    }
    u32x4 o0 = {pk_bf(yv[0][0], yv[0][1]), pk_bf(yv[0][2], yv[0][3]), pk_bf(yv[1][0], yv[1][1]), pk_bf(yv[1][2], yv[1][3])};
    u32x4 o1 = {pk_bf(yv[2][0], yv[2][1]), pk_bf(yv[2][2], yv[2][3]), pk_bf(yv[3][0], yv[3][1]), pk_bf(yv[3][2], yv[3][3])};
    u16* dst = yp + ((size_t)(t >> 1) * 256 + grp) * 32 + (t & 1) * 16;
    *(u32x4*)(dst) = o0;
    *(u32x4*)(dst + 8) = o1;
  }
  if (t >= 256 && t < 256 + 24) {
    const int n = t - 256;
    float s = 0.f;
#pragma unroll
    for (int i = 0; i < 16; ++i) s += *(const LDSP float*)(lbase + C_GP + (i * 32 + n) * 4);
    modout[grp * 24 + n] = (u16)(pk_bf(s, s) & 0xffffu);
  }
}

__device__ __forceinline__ float wave_sum(float x) {
  x += __uint_as_float(__builtin_amdgcn_update_dpp(0u, __float_as_uint(x), 0x111, 0xf, 0xf, true));
  x += __uint_as_float(__builtin_amdgcn_update_dpp(0u, __float_as_uint(x), 0x112, 0xf, 0xf, true));
  x += __uint_as_float(__builtin_amdgcn_update_dpp(0u, __float_as_uint(x), 0x114, 0xf, 0xf, true));
  x += __uint_as_float(__builtin_amdgcn_update_dpp(0u, __float_as_uint(x), 0x118, 0xf, 0xf, true));
  x += __uint_as_float(__builtin_amdgcn_update_dpp(0u, __float_as_uint(x), 0x142, 0xa, 0xf, true));
  x += __uint_as_float(__builtin_amdgcn_update_dpp(0u, __float_as_uint(x), 0x143, 0xc, 0xf, true));
  return __uint_as_float(__builtin_amdgcn_readlane(__float_as_uint(x), 63));
}

__global__ __launch_bounds__(256) void k1_norm(
    const u16* __restrict__ X, const u16* __restrict__ yp, const u16* __restrict__ mod,
    u16* __restrict__ x1out, u16* __restrict__ h2out) {
  const int t = threadIdx.x, lane = t & 63;
  const int w = __builtin_amdgcn_readfirstlane(t >> 6);
  const int rb = blockIdx.x;
  const int row0 = rb * 8 + 2 * w;
  u32x4 xq[2][2], yq[2][2], mq[5][2];
#pragma unroll
  for (int r = 0; r < 2; ++r)
#pragma unroll
    for (int hf = 0; hf < 2; ++hf)
      xq[r][hf] = *(const u32x4*)(X + (size_t)(row0 + r) * D_ + hf * 512 + lane * 8);
#pragma unroll
  for (int hf = 0; hf < 2; ++hf)
#pragma unroll
    for (int gg = 0; gg < 2; ++gg)
      yq[hf][gg] = *(const u32x4*)(yp + ((size_t)rb * 256 + hf * 128 + lane * 2 + gg) * 32 + w * 8);
#pragma unroll
  for (int m = 0; m < 5; ++m)
#pragma unroll
    for (int hf = 0; hf < 2; ++hf)
      mq[m][hf] = *(const u32x4*)(mod + m * 1024 + hf * 512 + lane * 8);

#pragma unroll
  for (int r = 0; r < 2; ++r) {
    float x[16];
#pragma unroll
    for (int i = 0; i < 8; ++i) {
      u32 v = xq[r][i >> 2][i & 3];
      x[2 * i] = bf_lo(v); x[2 * i + 1] = bf_hi(v);
    }
    float s = 0.f;
#pragma unroll
    for (int i = 0; i < 16; ++i) s += x[i];
    const float mean = wave_sum(s) * (1.0f / 1024.0f);
    float q = 0.f;
#pragma unroll
    for (int i = 0; i < 16; ++i) { float d = x[i] - mean; q = fmaf(d, d, q); }
    const float var = wave_sum(q) * (1.0f / 1024.0f);
    const float rstd = __builtin_amdgcn_rsqf(var + 1e-5f);
    float x1[16];
#pragma unroll
    for (int i = 0; i < 16; ++i) {
      const int hf = i >> 3, e = i & 7;
      u32 sa = mq[0][hf][e >> 1], ca = mq[1][hf][e >> 1], ga = mq[2][hf][e >> 1];
      float saf = (e & 1) ? bf_hi(sa) : bf_lo(sa);
      float caf = (e & 1) ? bf_hi(ca) : bf_lo(ca);
      float gaf = (e & 1) ? bf_hi(ga) : bf_lo(ga);
      u32 yv = yq[hf][e >> 2][r * 2 + ((e >> 1) & 1)];
      float yf = (e & 1) ? bf_hi(yv) : bf_lo(yv);
      float h = fmaf((x[i] - mean) * rstd, 1.0f + caf, saf);
      x1[i] = fmaf(gaf, h * yf, x[i]);
    }
    u32x4 xo[2];
#pragma unroll
    for (int i = 0; i < 8; ++i) {
      u32 p = pk_bf(x1[2 * i], x1[2 * i + 1]);
      xo[i >> 2][i & 3] = p;
      x1[2 * i] = bf_lo(p); x1[2 * i + 1] = bf_hi(p);
    }
#pragma unroll
    for (int hf = 0; hf < 2; ++hf)
      *(u32x4*)(x1out + (size_t)(row0 + r) * D_ + hf * 512 + lane * 8) = xo[hf];
    float s2 = 0.f;
#pragma unroll
    for (int i = 0; i < 16; ++i) s2 += x1[i];
    const float mean2 = wave_sum(s2) * (1.0f / 1024.0f);
    float q2 = 0.f;
#pragma unroll
    for (int i = 0; i < 16; ++i) { float d = x1[i] - mean2; q2 = fmaf(d, d, q2); }
    const float var2 = wave_sum(q2) * (1.0f / 1024.0f);
    const float rstd2 = __builtin_amdgcn_rsqf(var2 + 1e-5f);
    u32x4 ho[2];
#pragma unroll
    for (int i = 0; i < 8; ++i) {
      const int hf = i >> 2, e = i & 3;
      u32 sb = mq[3][hf][e], cb = mq[4][hf][e];
      float h0 = fmaf((x1[2 * i] - mean2) * rstd2, 1.0f + bf_lo(cb), bf_lo(sb));
      float h1 = fmaf((x1[2 * i + 1] - mean2) * rstd2, 1.0f + bf_hi(cb), bf_hi(sb));
      ho[hf][e] = pk_bf(h0, h1);
    }
    const int row = row0 + r, mt = row >> 4, mr = row & 15;
#pragma unroll
    for (int hf = 0; hf < 2; ++hf)
      *(u32x4*)((char*)h2out + (size_t)mt * 32768 + (hf * 64 + lane) * 256 + mr * 16) = ho[hf];
  }
}

constexpr int G1_R = 5;
constexpr int G1_WOFF = 16384;
constexpr int G1_UP = 7168 + 16;
constexpr int G1_SLOT = 16384 + G1_UP + 7168;
constexpr int G1_NK = 32;

struct G1Frags { bf16x8 x[4]; bf16x8 wg[3], wu[3], wm; };

__global__ __launch_bounds__(512, 2) void k2_gemm1(
    const u16* __restrict__ h2, const u16* __restrict__ w1, const u16* __restrict__ w3,
    u16* __restrict__ hh) {
  extern __shared__ __attribute__((aligned(16))) char smem[];
  const int t = threadIdx.x, lane = t & 63;
  const int w = __builtin_amdgcn_readfirstlane(t >> 6);
  const int b = blockIdx.x, xcd = b & 7, idx = b >> 3;
  const int mblk = (xcd & 1) * 4 + (idx & 3), nblk = (xcd >> 1) * 8 + (idx >> 2);
  const int wm = w & 3, wn = w >> 2;
  const u32 lbase = (u32)(uintptr_t)(LDSP char*)smem;

  const char* xsrc = (const char*)h2 + (size_t)(mblk * 16 + 2 * w) * 32768;
  const char* wsrc = (const char*)((w >> 2) ? w3 : w1) + nblk * 224;
  const u32 xv0 = lane * 16, xv1 = lane * 16 + 32768;
  u32 wv0, wv1;
  {
    int c0 = lane, c1 = lane + 64;
    if (c1 > 111) c1 = 111;
    int p0 = 8 * (w & 3) + c0 / 14, p1 = 8 * (w & 3) + c1 / 14;
    int k0 = 8 * ((p0 >> 2) & 3) + 4 * (p0 >> 4) + (p0 & 3);
    int k1 = 8 * ((p1 >> 2) & 3) + 4 * (p1 >> 4) + (p1 & 3);
    wv0 = k0 * 7168 + (c0 % 14) * 16;
    wv1 = k1 * 7168 + (c1 % 14) * 16;
  }
  const u32 xdst = lbase + 2 * w * 1024;
  const u32 wdst = lbase + G1_WOFF + (w >> 2) * G1_UP + (w & 3) * 1792;
  auto issue = [&](int kb, u32 boff) {
    const char* xs = xsrc + kb * 1024;
    const char* ws = wsrc + (size_t)kb * (32 * 7168);
    glds16(xs, xv0, xdst + boff);
    glds16(xs, xv1, xdst + boff + 1024);
    glds16(ws, wv0, wdst + boff);
    glds16_48(ws, wv1, wdst + boff + 1024);
  };
  const int g = lane >> 4, q = (lane >> 2) & 3, p = lane & 3;
  const u32 xr = lbase + wm * 4096 + lane * 16;
  const u32 wr = lbase + G1_WOFF + (4 * g + q) * 224 + 8 * p + wn * 128;
  const u32 wrm = lbase + G1_WOFF + (4 * g + q) * 224 + (p & 1) * G1_UP + (48 + 8 * wn + 4 * (p >> 1)) * 2;
  auto rd = [&](G1Frags& f, u32 boff) {
#pragma unroll
    for (int i = 0; i < 4; ++i) f.x[i] = lds_b128(xr + boff + i * 1024);
#pragma unroll
    for (int i = 0; i < 3; ++i) {
      f.wg[i] = lds_tr2(wr + boff + 32 * i, wr + boff + 32 * i + 3584);
      f.wu[i] = lds_tr2(wr + boff + G1_UP + 32 * i, wr + boff + G1_UP + 32 * i + 3584);
    }
    f.wm = lds_tr2(wrm + boff, wrm + boff + 3584);
  };
  f32x4 ag[4][3], au[4][3], am[4];
#pragma unroll
  for (int m = 0; m < 4; ++m) {
#pragma unroll
    for (int i = 0; i < 3; ++i) { ag[m][i] = (f32x4){0.f, 0.f, 0.f, 0.f}; au[m][i] = (f32x4){0.f, 0.f, 0.f, 0.f}; }
    am[m] = (f32x4){0.f, 0.f, 0.f, 0.f};
  }
  auto mm = [&](const G1Frags& f) {
#pragma unroll
    for (int m = 0; m < 4; ++m) {
#pragma unroll
      for (int i = 0; i < 3; ++i) {
        ag[m][i] = mfma16(f.wg[i], f.x[m], ag[m][i]);
        au[m][i] = mfma16(f.wu[i], f.x[m], au[m][i]);
      }
      am[m] = mfma16(f.wm, f.x[m], am[m]);
    }
  };

  G1Frags fa, fb;
#pragma unroll
  for (int s = 0; s < G1_R; ++s) issue(s, s * G1_SLOT);
  WAIT_VM(4 * (G1_R - 1));
  BARRIER();
  rd(fa, 0);
  u32 cur = 0;
  auto nxt = [&](u32 o) { u32 n = o + G1_SLOT; return n == G1_R * G1_SLOT ? 0u : n; };
#pragma unroll 1
  for (int tt = 0; tt < G1_NK - G1_R - 1; tt += 2) {
    u32 n1 = nxt(cur), n2 = nxt(n1);
    WAIT_VM(4 * (G1_R - 2)); BARRIER();
    issue(tt + G1_R, cur);
    rd(fb, n1);
    mm(fa);
    WAIT_VM(4 * (G1_R - 2)); BARRIER();
    issue(tt + 1 + G1_R, n1);
    rd(fa, n2);
    mm(fb);
    cur = n2;
  }
  {
    u32 n1 = nxt(cur), n2 = nxt(n1), n3 = nxt(n2), n4 = nxt(n3);
    WAIT_VM(12); BARRIER();
    issue(G1_NK - 1, cur);
    rd(fb, n1); mm(fa);
    WAIT_VM(12); BARRIER();
    rd(fa, n2); mm(fb);
    WAIT_VM(8); BARRIER();
    rd(fb, n3); mm(fa);
    WAIT_VM(4); BARRIER();
    rd(fa, n4); mm(fb);
    WAIT_VM(0); BARRIER();
    rd(fb, cur); mm(fa);
    mm(fb);
  }

  const int m16 = lane & 15;
  char* hbase = (char*)hh + (size_t)(mblk * 16 + wm * 4) * (112 * 1024);
  const int f0 = nblk * 112 + wn * 64;
#pragma unroll
  for (int m = 0; m < 4; ++m) {
#pragma unroll
    for (int i = 0; i < 3; ++i) {
      float o[4];
#pragma unroll
      for (int r = 0; r < 4; ++r) o[r] = silu_f(ag[m][i][r]) * au[m][i][r];
      const int f = f0 + 16 * i + 4 * g;
      *(u32x2*)(hbase + (size_t)m * (112 * 1024) + (f >> 3) * 256 + m16 * 16 + (f & 4) * 2) =
          (u32x2){pk_bf(o[0], o[1]), pk_bf(o[2], o[3])};
    }
  }
#pragma unroll
  for (int mp = 0; mp < 2; ++mp) {
    float o[4];
#pragma unroll
    for (int r = 0; r < 4; ++r) {
      auto sw = __builtin_amdgcn_permlane16_swap(__float_as_uint(am[2 * mp][r]), __float_as_uint(am[2 * mp + 1][r]), false, false);
      o[r] = silu_f(__uint_as_float(sw[0])) * __uint_as_float(sw[1]);
    }
    const int f = nblk * 112 + 48 + 8 * wn + 4 * (g >> 1);
    const int m = 2 * mp + (g & 1);
    *(u32x2*)(hbase + (size_t)m * (112 * 1024) + (f >> 3) * 256 + m16 * 16 + (f & 4) * 2) =
        (u32x2){pk_bf(o[0], o[1]), pk_bf(o[2], o[3])};
  }
}

constexpr int G2_R = 6;
constexpr int G2_WOFF = 16384;
constexpr int G2_SLOT = 16384 + 8192;
constexpr int G2_NS = 56;

struct G2Frags { bf16x8 x[2]; bf16x8 w[4]; };

__global__ __launch_bounds__(512, 2) void k3_gemm2(
    const u16* __restrict__ hh, const u16* __restrict__ w2, const u16* __restrict__ x1,
    const u16* __restrict__ mod, u16* __restrict__ out) {
  extern __shared__ __attribute__((aligned(16))) char smem[];
  const int t = threadIdx.x, lane = t & 63;
  const int w = __builtin_amdgcn_readfirstlane(t >> 6);
  const int b = blockIdx.x, xcd = b & 7, idx = b >> 3;
  const int mblk = (xcd & 3) * 4 + (idx & 3), nblk = (xcd >> 2) * 8 + (idx >> 2);
  const int wm = w & 3, wk = w >> 2;
  const u32 lbase = (u32)(uintptr_t)(LDSP char*)smem;

  const char* xsrc = (const char*)hh + (size_t)(mblk * 8 + w) * (112 * 1024);
  const char* wsrc = (const char*)w2 + nblk * 128;
  const u32 xv0 = lane * 16, xv1 = lane * 16 + 1024;
  u32 wv;
  {
    int pos = 8 * w + (lane >> 3);
    int pp = pos & 31;
    int k = (pos & 32) + 8 * ((pp >> 2) & 3) + 4 * (pp >> 4) + (pp & 3);
    int cc = (lane & 7) ^ (((pos >> 1) & 3) << 1);
    wv = k * 2048 + cc * 16;
  }
  const u32 xdst = lbase + w * 2048;
  const u32 wdst = lbase + G2_WOFF + w * 1024;
  auto issue = [&](int s, u32 boff) {
    const char* xs = xsrc + s * 2048;
    const char* ws = wsrc + (size_t)s * (64 * 2048);
    glds16(xs, xv0, xdst + boff);
    glds16(xs, xv1, xdst + boff + 1024);
    glds16(ws, wv, wdst + boff);
  };
  const int g = lane >> 4, q = (lane >> 2) & 3, p = lane & 3;
  const u32 xr = lbase + wm * 4096 + wk * 1024 + lane * 16;
  const int key = (2 * g + (q >> 1)) & 3;
  u32 wra[4];
#pragma unroll
  for (int nt = 0; nt < 4; ++nt)
    wra[nt] = lbase + G2_WOFF + wk * 4096 + (4 * g + q) * 128 + ((nt ^ key) * 32) + 8 * p;
  auto rd = [&](G2Frags& f, u32 boff) {
    f.x[0] = lds_b128(xr + boff);
    f.x[1] = lds_b128(xr + boff + 2048);
#pragma unroll
    for (int nt = 0; nt < 4; ++nt) f.w[nt] = lds_tr2(wra[nt] + boff, wra[nt] + boff + 2048);
  };
  f32x4 acc[2][4];
#pragma unroll
  for (int m = 0; m < 2; ++m)
#pragma unroll
    for (int nt = 0; nt < 4; ++nt) acc[m][nt] = (f32x4){0.f, 0.f, 0.f, 0.f};
  auto mm = [&](const G2Frags& f) {
#pragma unroll
    for (int m = 0; m < 2; ++m)
#pragma unroll
      for (int nt = 0; nt < 4; ++nt) acc[m][nt] = mfma16(f.w[nt], f.x[m], acc[m][nt]);
  };
  G2Frags fa, fb;
#pragma unroll
  for (int s = 0; s < G2_R; ++s) issue(s, s * G2_SLOT);
  WAIT_VM(3 * (G2_R - 1));
  BARRIER();
  rd(fa, 0);
  u32 cur = 0;
  auto nxt = [&](u32 o) { u32 n = o + G2_SLOT; return n == G2_R * G2_SLOT ? 0u : n; };
#pragma unroll 1
  for (int tt = 0; tt < G2_NS - G2_R; tt += 2) {
    u32 n1 = nxt(cur), n2 = nxt(n1);
    WAIT_VM(3 * (G2_R - 2)); BARRIER();
    issue(tt + G2_R, cur);
    rd(fb, n1);
    mm(fa);
    WAIT_VM(3 * (G2_R - 2)); BARRIER();
    issue(tt + 1 + G2_R, n1);
    rd(fa, n2);
    mm(fb);
    cur = n2;
  }
  const int m16 = lane & 15;
  const int row = mblk * 128 + (2 * wm + wk) * 16 + m16;
  const int col0 = nblk * 64 + 4 * g;
  {
    u32 n1 = nxt(cur), n2 = nxt(n1), n3 = nxt(n2), n4 = nxt(n3), n5 = nxt(n4);
    WAIT_VM(12); BARRIER();
    rd(fb, n1); mm(fa);
    WAIT_VM(9); BARRIER();
    rd(fa, n2); mm(fb);
    WAIT_VM(6); BARRIER();
    rd(fb, n3); mm(fa);
    WAIT_VM(3); BARRIER();
    rd(fa, n4); mm(fb);
    WAIT_VM(0); BARRIER();
    rd(fb, n5); mm(fa);
    mm(fb);
  }
  u32x2 xq[4], gq[4];
#pragma unroll
  for (int nt = 0; nt < 4; ++nt) {
    xq[nt] = *(const u32x2*)(x1 + (size_t)row * D_ + col0 + 16 * nt);
    gq[nt] = *(const u32x2*)(mod + 5 * 1024 + col0 + 16 * nt);
  }
  BARRIER();
#pragma unroll
  for (int nt = 0; nt < 4; ++nt)
    *(LDSP f32x4*)(lbase + (w * 4 + nt) * 1024 + lane * 16) = wk ? acc[0][nt] : acc[1][nt];
  BARRIER();
#pragma unroll
  for (int nt = 0; nt < 4; ++nt) {
    f32x4 o = *(const LDSP f32x4*)(lbase + ((w ^ 4) * 4 + nt) * 1024 + lane * 16);
    f32x4 mine = wk ? acc[1][nt] : acc[0][nt];
    o += mine;
    u32 b0 = pk_bf(o[0], o[1]), b1 = pk_bf(o[2], o[3]);
    float r0 = fmaf(bf_lo(gq[nt][0]), bf_lo(b0), bf_lo(xq[nt][0]));
    float r1 = fmaf(bf_hi(gq[nt][0]), bf_hi(b0), bf_hi(xq[nt][0]));
    float r2 = fmaf(bf_lo(gq[nt][1]), bf_lo(b1), bf_lo(xq[nt][1]));
    float r3 = fmaf(bf_hi(gq[nt][1]), bf_hi(b1), bf_hi(xq[nt][1]));
    *(u32x2*)(out + (size_t)row * D_ + col0 + 16 * nt) = (u32x2){pk_bf(r0, r1), pk_bf(r2, r3)};
  }
}

extern "C" void kernel_launch(void* const* d_in, const int* in_sizes, int n_in,
                              void* d_out, int out_size, void* d_ws, size_t ws_size,
                              hipStream_t stream) {
  const u16* x = (const u16*)d_in[0];
  const u16* kin = (const u16*)d_in[1];
  const u16* vin = (const u16*)d_in[2];
  const u16* shk = (const u16*)d_in[3];
  const u16* ssmk = (const u16*)d_in[4];
  const u16* ssmD = (const u16*)d_in[5];
  const u16* w1 = (const u16*)d_in[7];
  const u16* w3 = (const u16*)d_in[8];
  const u16* w2 = (const u16*)d_in[9];
  const u16* cvec = (const u16*)d_in[10];
  const u16* wmod = (const u16*)d_in[11];
  char* ws = (char*)d_ws;
  u16* mod = (u16*)(ws + WS_MOD);
  u16* yp = (u16*)(ws + WS_YP);
  u16* x1 = (u16*)(ws + WS_X1);
  u16* h2 = (u16*)(ws + WS_H2);
  u16* hh = (u16*)(ws + WS_HH);
  (void)hipFuncSetAttribute((const void*)k0_conv, hipFuncAttributeMaxDynamicSharedMemorySize, C_LDS);
  (void)hipFuncSetAttribute((const void*)k2_gemm1, hipFuncAttributeMaxDynamicSharedMemorySize, G1_R * G1_SLOT);
  (void)hipFuncSetAttribute((const void*)k3_gemm2, hipFuncAttributeMaxDynamicSharedMemorySize, G2_R * G2_SLOT);
  k0_conv<<<256, 512, C_LDS, stream>>>(kin, vin, shk, ssmk, ssmD, cvec, wmod, mod, yp);
  k1_norm<<<256, 256, 0, stream>>>(x, yp, mod, x1, h2);
  k2_gemm1<<<256, 512, G1_R * G1_SLOT, stream>>>(h2, w1, w3, hh);
  k3_gemm2<<<256, 512, G2_R * G2_SLOT, stream>>>(hh, w2, x1, mod, (u16*)d_out);
}
```

```cpp
#include <hip/hip_runtime.h>
#include <stdint.h>

typedef unsigned short u16;
typedef unsigned int u32;
typedef short s16x4 __attribute__((ext_vector_type(4)));
typedef short s16x8 __attribute__((ext_vector_type(8)));
typedef __bf16 bf16x2 __attribute__((ext_vector_type(2)));
typedef __bf16 bf16x8 __attribute__((ext_vector_type(8)));
typedef float f32x2 __attribute__((ext_vector_type(2)));
typedef float f32x4 __attribute__((ext_vector_type(4)));
typedef u32 u32x2 __attribute__((ext_vector_type(2)));
typedef u32 u32x4 __attribute__((ext_vector_type(4)));
#define LDSP __attribute__((address_space(3)))

constexpr int S_ = 2048, D_ = 1024, F_ = 3584;

constexpr size_t WS_MOD = 0;
constexpr size_t WS_YP = 1u << 20;
constexpr size_t WS_X1 = WS_YP + (4u << 20);
constexpr size_t WS_H2 = WS_X1 + (4u << 20);
constexpr size_t WS_HH = WS_H2 + (4u << 20);

__device__ __forceinline__ float bf_lo(u32 v) { return __uint_as_float(v << 16); }
__device__ __forceinline__ float bf_hi(u32 v) { return __uint_as_float(v & 0xffff0000u); }
__device__ __forceinline__ u32 pk_bf(float lo, float hi) {
  f32x2 f = {lo, hi};
  bf16x2 b = __builtin_convertvector(f, bf16x2);
  return __builtin_bit_cast(u32, b);
}
__device__ __forceinline__ float rbf(float x) { return bf_lo(pk_bf(x, x)); }
__device__ __forceinline__ float silu_f(float x) {
  return x * __builtin_amdgcn_rcpf(1.0f + __builtin_amdgcn_exp2f(x * -1.44269504088896341f));
}
__device__ __forceinline__ f32x4 mfma16(bf16x8 a, bf16x8 b, f32x4 c) {
  return __builtin_amdgcn_mfma_f32_16x16x32_bf16(a, b, c, 0, 0, 0);
}
__device__ __forceinline__ void glds16(const void* sbase, u32 voff, u32 ldsoff) {
  asm volatile("s_mov_b32 m0, %2\n\ts_nop 0\n\tglobal_load_lds_dwordx4 %0, %1"
               :: "v"(voff), "s"(sbase), "s"(ldsoff) : "memory");
}
__device__ __forceinline__ void glds16_48(const void* sbase, u32 voff, u32 ldsoff) {
  asm volatile("s_mov_b32 m0, %2\n\ts_mov_b32 exec_hi, 0xffff\n\tglobal_load_lds_dwordx4 %0, %1\n\ts_mov_b32 exec_hi, -1"
               :: "v"(voff), "s"(sbase), "s"(ldsoff) : "memory");
}
__device__ __forceinline__ void glds4(const void* sbase, u32 voff, u32 ldsoff) {
  asm volatile("s_mov_b32 m0, %2\n\ts_nop 0\n\tglobal_load_lds_dword %0, %1"
               :: "v"(voff), "s"(sbase), "s"(ldsoff) : "memory");
}
#define SCHED0() __builtin_amdgcn_sched_barrier(0)
#define WAIT_VM(n) do { SCHED0(); asm volatile("s_waitcnt vmcnt(%0)" :: "n"(n) : "memory"); } while (0)
#define BARRIER() do { asm volatile("s_waitcnt lgkmcnt(0)\n\ts_barrier" ::: "memory"); SCHED0(); } while (0)

#define STAMP_DECL
#define STAMP(i)
#define STAMP_END(kid)

__device__ __forceinline__ bf16x8 lds_b128(u32 off) {
  return *(const LDSP bf16x8*)(off);
}
__device__ __forceinline__ bf16x8 lds_tr2(u32 off0, u32 off1) {
  s16x4 a = __builtin_amdgcn_ds_read_tr16_b64_v4i16((LDSP s16x4*)(off0));
  s16x4 b = __builtin_amdgcn_ds_read_tr16_b64_v4i16((LDSP s16x4*)(off1));
  s16x8 ab = __builtin_shufflevector(a, b, 0, 1, 2, 3, 4, 5, 6, 7);
  return __builtin_bit_cast(bf16x8, ab);
}

constexpr int C_KPAD = 448;
constexpr int C_KE = C_KPAD;
constexpr int C_KO = 4576 + C_KPAD;
constexpr int C_R1 = 9152;
constexpr int C_R0 = C_R1 + 4224;
constexpr int C_CH = 9152 + 8384;
constexpr int C_GP = 4 * C_CH;
constexpr int C_LDS = C_GP + 2048;

__global__ __launch_bounds__(512, 2) void k0_conv(
    const u16* __restrict__ Kin, const u16* __restrict__ Vin, const u16* __restrict__ shk,
    const u16* __restrict__ ssmk, const u16* __restrict__ ssmD, const u16* __restrict__ cvec,
    const u16* __restrict__ wmod, u16* __restrict__ modout, u16* __restrict__ yp) {
  extern __shared__ __attribute__((aligned(16))) char smem[];
  STAMP_DECL
  const int t = threadIdx.x, lane = t & 63;
  const int w = __builtin_amdgcn_readfirstlane(t >> 6);
  const int b = blockIdx.x;
  const int grp = (b & 7) * 32 + (b >> 3);
  const int d0 = grp * 4;
  const u32 lbase = (u32)(uintptr_t)(LDSP char*)smem;

  const u16* fk = ssmk + (size_t)d0 * S_;
  u32x4 fc[2]; u32 fp[2];
#pragma unroll
  for (int i = 0; i < 2; ++i) {
    int z = t + 512 * i;
    fc[i] = *(const u32x4*)(fk + z * 8);
    int zz = (z & 255) ? z : z + 1;
    fp[i] = *(const u32*)(fk + zz * 8 - 2);
  }
  u32x2 kr[4], vr[4], hk[3];
  const u16* kp = Kin + (size_t)(4 * t) * D_ + d0;
  const u16* vp = Vin + (size_t)(4 * t) * D_ + d0;
#pragma unroll
  for (int r = 0; r < 4; ++r) kr[r] = *(const u32x2*)(kp + r * D_);
#pragma unroll
  for (int r = 0; r < 4; ++r) vr[r] = *(const u32x2*)(vp + r * D_);
#pragma unroll
  for (int r = 0; r < 3; ++r) hk[r] = (u32x2){0u, 0u};
  if (lane == 0 && t != 0) {
#pragma unroll
    for (int r = 0; r < 3; ++r) hk[r] = *(const u32x2*)(kp + (r - 3) * D_);
  }
  u32x4 wq[16]; u32x4 cq[2];
  const int gq = lane & 3, rho = lane >> 2;
  if (w >= 4) {
    const int row0 = (w - 4) * 256 + rho * 16;
    cq[0] = *(const u32x4*)(cvec + row0);
    cq[1] = *(const u32x4*)(cvec + row0 + 8);
    const u16* wp = wmod + (size_t)row0 * 6144 + grp * 24 + (gq < 3 ? gq : 2) * 8;
#pragma unroll
    for (int i = 0; i < 16; ++i) wq[i] = *(const u32x4*)(wp + (size_t)i * 6144);
  }
  const u32x4 sk01 = *(const u32x4*)(shk + d0 * 4);
  const u32x4 sk23 = *(const u32x4*)(shk + d0 * 4 + 8);
  const u32x2 dD = *(const u32x2*)(ssmD + d0);

  if (t < 224) {
    int ch = t / 56, rem = t % 56, cp = rem / 28, i = rem % 28;
    *(LDSP u32x4*)(lbase + ch * C_CH + cp * 4576 + i * 16) = (u32x4){0u, 0u, 0u, 0u};
  } else if (t >= 256 && t < 256 + 64) {
    int i = t - 256, ch = i >> 4, cp = (i >> 3) & 1, j = i & 7;
    *(LDSP u32x2*)(lbase + ch * C_CH + (cp ? C_R0 : C_R1) + 4096 + j * 8) = (u32x2){0u, 0u};
  } else if (t >= 320 && t < 324) {
    *(LDSP u32*)(lbase + (t - 320) * C_CH + C_R0 + 4092) = 0u;
  }
#pragma unroll
  for (int i = 0; i < 2; ++i) {
    int z = t + 512 * i, ch = z >> 8, zc = z & 255;
    u32 prev = zc ? fp[i] : 0u;
    u32x4 e;
    e[0] = __builtin_amdgcn_alignbit(fc[i][0], prev, 16);
    e[1] = __builtin_amdgcn_alignbit(fc[i][1], fc[i][0], 16);
    e[2] = __builtin_amdgcn_alignbit(fc[i][2], fc[i][1], 16);
    e[3] = __builtin_amdgcn_alignbit(fc[i][3], fc[i][2], 16);
    *(LDSP u32x4*)(lbase + ch * C_CH + C_KE + zc * 16) = e;
    *(LDSP u32x4*)(lbase + ch * C_CH + C_KO + zc * 16) = fc[i];
  }
  STAMP(2);
  u32 upk[4][2];
  {
#pragma unroll
    for (int r = 0; r < 3; ++r) {
      hk[r][0] = __builtin_amdgcn_update_dpp(hk[r][0], kr[r + 1][0], 0x138, 0xf, 0xf, false);
      hk[r][1] = __builtin_amdgcn_update_dpp(hk[r][1], kr[r + 1][1], 0x138, 0xf, 0xf, false);
    }
#pragma unroll
    for (int ch = 0; ch < 4; ++ch) {
      const u32x4 sk = ch < 2 ? sk01 : sk23;
      const u32 s0 = sk[(ch & 1) * 2], s1 = sk[(ch & 1) * 2 + 1];
      const float w0 = bf_lo(s0), w1 = bf_hi(s0), w2 = bf_lo(s1), w3 = bf_hi(s1);
      float kf[7];
#pragma unroll
      for (int r = 0; r < 7; ++r) {
        u32 dw = r < 3 ? hk[r][ch >> 1] : kr[r - 3][ch >> 1];
        kf[r] = (ch & 1) ? bf_hi(dw) : bf_lo(dw);
      }
      float u[4];
#pragma unroll
      for (int r = 0; r < 4; ++r) {
        float a = kf[r] * w3;
        a = fmaf(kf[r + 1], w2, a);
        a = fmaf(kf[r + 2], w1, a);
        a = fmaf(kf[r + 3], w0, a);
        u32 dv = vr[r][ch >> 1];
        u[r] = a * ((ch & 1) ? bf_hi(dv) : bf_lo(dv));
      }
      upk[ch][0] = pk_bf(u[3], u[2]);
      upk[ch][1] = pk_bf(u[1], u[0]);
      *(LDSP u32x2*)(lbase + ch * C_CH + C_R1 + (2044 - 4 * t) * 2) = (u32x2){upk[ch][0], upk[ch][1]};
      LDSP u32* r0 = (LDSP u32*)(lbase + ch * C_CH + C_R0 + (2042 - 4 * t) * 2);
      r0[0] = upk[ch][0];
      r0[1] = upk[ch][1];
    }
  }
  STAMP(3);
  __syncthreads();
  STAMP(4);

  if (w < 4) {
    const int g = lane >> 4, i16 = lane & 15;
    const u32 cb = lbase + w * C_CH;
    u32 la = cb + ((i16 & 1) ? C_KO : C_KE) + 64 * (i16 >> 1) + 16 * g;
    u32 lb = cb + ((i16 & 1) ? C_R1 : C_R0) + 4088 - 8 * (i16 >> 1) + 16 * g - 3584;
    f32x4 acc[8];
#pragma unroll
    for (int c = 0; c < 8; ++c) acc[c] = (f32x4){0.f, 0.f, 0.f, 0.f};
#pragma unroll 2
    for (int qp = 0; qp < 8; ++qp) {
      bf16x8 A[8], B[8];
#pragma unroll
      for (int e = 0; e < 8; ++e) {
        u32x2 lo = *(volatile const LDSP u32x2*)(lb + 512 * (7 - e));
        u32x2 hi = *(volatile const LDSP u32x2*)(lb + 512 * (7 - e) + 8);
        u32x4 v = {lo[0], lo[1], hi[0], hi[1]};
        B[e] = __builtin_bit_cast(bf16x8, v);
        A[e] = lds_b128(la + 512 * e);
      }
#pragma unroll
      for (int a = 0; a < 8; ++a)
#pragma unroll
        for (int e = 0; e < 8 - a; ++e) acc[a + e] = mfma16(A[a], B[e], acc[a + e]);
      la -= 64; lb -= 64;
    }
#pragma unroll
    for (int c = 0; c < 8; ++c) {
      u32 o = cb + (256 * c + 64 * g + 2 * i16) * 2;
      *(LDSP u32*)(o) = pk_bf(acc[c][0], acc[c][1]);
      *(LDSP u32*)(o + 64) = pk_bf(acc[c][2], acc[c][3]);
    }
  } else {
    u32 sc[8];
#pragma unroll
    for (int i = 0; i < 8; ++i) {
      u32 cv = cq[i >> 2][i & 3];
      sc[i] = pk_bf(silu_f(bf_lo(cv)), silu_f(bf_hi(cv)));
    }
    float ga[8];
#pragma unroll
    for (int j = 0; j < 8; ++j) ga[j] = 0.f;
#pragma unroll
    for (int i = 0; i < 8; ++i) {
#pragma unroll
      for (int j = 0; j < 4; ++j) {
        u32 a = wq[2 * i][j], bb = wq[2 * i + 1][j];
        u32 lo = __builtin_amdgcn_perm(bb, a, 0x05040100u);
        u32 hi = __builtin_amdgcn_perm(bb, a, 0x07060302u);
        ga[2 * j] = __builtin_amdgcn_fdot2_f32_bf16(__builtin_bit_cast(bf16x2, lo), __builtin_bit_cast(bf16x2, sc[i]), ga[2 * j], false);
        ga[2 * j + 1] = __builtin_amdgcn_fdot2_f32_bf16(__builtin_bit_cast(bf16x2, hi), __builtin_bit_cast(bf16x2, sc[i]), ga[2 * j + 1], false);
      }
    }
#pragma unroll
    for (int j = 0; j < 8; ++j) {
      ga[j] += __uint_as_float(__builtin_amdgcn_update_dpp(0u, __float_as_uint(ga[j]), 0x114, 0xf, 0xf, true));
      ga[j] += __uint_as_float(__builtin_amdgcn_update_dpp(0u, __float_as_uint(ga[j]), 0x118, 0xf, 0xf, true));
    }
    if ((lane & 15) >= 12) {
      u32 o = lbase + C_GP + (((w - 4) * 4 + (lane >> 4)) * 32 + gq * 8) * 4;
      *(LDSP f32x4*)(o) = (f32x4){ga[0], ga[1], ga[2], ga[3]};
      *(LDSP f32x4*)(o + 16) = (f32x4){ga[4], ga[5], ga[6], ga[7]};
    }
  }
  STAMP(5);
  __syncthreads();

  {
    float yv[4][4];
#pragma unroll
    for (int ch = 0; ch < 4; ++ch) {
      u32x2 y = *(const LDSP u32x2*)(lbase + ch * C_CH + 8 * t);
      const float Dv = (ch & 1) ? bf_hi(dD[ch >> 1]) : bf_lo(dD[ch >> 1]);
      u32 p0 = pk_bf(bf_hi(upk[ch][1]) * Dv, bf_lo(upk[ch][1]) * Dv);
      u32 p1 = pk_bf(bf_hi(upk[ch][0]) * Dv, bf_lo(upk[ch][0]) * Dv);
      yv[0][ch] = bf_lo(y[0]) + bf_lo(p0);
      yv[1][ch] = bf_hi(y[0]) + bf_hi(p0);
      yv[2][ch] = bf_lo(y[1]) + bf_lo(p1);
      yv[3][ch] = bf_hi(y[1]) + bf_hi(p1);
    }
    u32x4 o0 = {pk_bf(yv[0][0], yv[0][1]), pk_bf(yv[0][2], yv[0][3]), pk_bf(yv[1][0], yv[1][1]), pk_bf(yv[1][2], yv[1][3])};
    u32x4 o1 = {pk_bf(yv[2][0], yv[2][1]), pk_bf(yv[2][2], yv[2][3]), pk_bf(yv[3][0], yv[3][1]), pk_bf(yv[3][2], yv[3][3])};
    u16* dst = yp + ((size_t)(t >> 1) * 256 + grp) * 32 + (t & 1) * 16;
    *(u32x4*)(dst) = o0;
    *(u32x4*)(dst + 8) = o1;
  }
  if (t >= 256 && t < 256 + 24) {
    const int n = t - 256;
    float s = 0.f;
#pragma unroll
    for (int i = 0; i < 16; ++i) s += *(const LDSP float*)(lbase + C_GP + (i * 32 + n) * 4);
    modout[grp * 24 + n] = (u16)(pk_bf(s, s) & 0xffffu);
  }
  STAMP_END(0);
}

__device__ __forceinline__ float wave_sum(float x) {
  x += __uint_as_float(__builtin_amdgcn_update_dpp(0u, __float_as_uint(x), 0x111, 0xf, 0xf, true));
  x += __uint_as_float(__builtin_amdgcn_update_dpp(0u, __float_as_uint(x), 0x112, 0xf, 0xf, true));
  x += __uint_as_float(__builtin_amdgcn_update_dpp(0u, __float_as_uint(x), 0x114, 0xf, 0xf, true));
  x += __uint_as_float(__builtin_amdgcn_update_dpp(0u, __float_as_uint(x), 0x118, 0xf, 0xf, true));
  x += __uint_as_float(__builtin_amdgcn_update_dpp(0u, __float_as_uint(x), 0x142, 0xa, 0xf, true));
  x += __uint_as_float(__builtin_amdgcn_update_dpp(0u, __float_as_uint(x), 0x143, 0xc, 0xf, true));
  return __uint_as_float(__builtin_amdgcn_readlane(__float_as_uint(x), 63));
}

__global__ __launch_bounds__(256) void k1_norm(
    const u16* __restrict__ X, const u16* __restrict__ yp, const u16* __restrict__ mod,
    u16* __restrict__ x1out, u16* __restrict__ h2out) {
  const int t = threadIdx.x, lane = t & 63;
  const int w = __builtin_amdgcn_readfirstlane(t >> 6);
  STAMP_DECL
  const int rb = blockIdx.x;
  const int row0 = rb * 8 + 2 * w;
  u32x4 xq[2][2], yq[2][2], mq[5][2];
#pragma unroll
  for (int r = 0; r < 2; ++r)
#pragma unroll
    for (int hf = 0; hf < 2; ++hf)
      xq[r][hf] = *(const u32x4*)(X + (size_t)(row0 + r) * D_ + hf * 512 + lane * 8);
#pragma unroll
  for (int hf = 0; hf < 2; ++hf)
#pragma unroll
    for (int gg = 0; gg < 2; ++gg)
      yq[hf][gg] = *(const u32x4*)(yp + ((size_t)rb * 256 + hf * 128 + lane * 2 + gg) * 32 + w * 8);
#pragma unroll
  for (int m = 0; m < 5; ++m)
#pragma unroll
    for (int hf = 0; hf < 2; ++hf)
      mq[m][hf] = *(const u32x4*)(mod + m * 1024 + hf * 512 + lane * 8);

#pragma unroll
  for (int r = 0; r < 2; ++r) {
    float x[16];
#pragma unroll
    for (int i = 0; i < 8; ++i) {
      u32 v = xq[r][i >> 2][i & 3];
      x[2 * i] = bf_lo(v); x[2 * i + 1] = bf_hi(v);
    }
    float s = 0.f;
#pragma unroll
    for (int i = 0; i < 16; ++i) s += x[i];
    const float mean = wave_sum(s) * (1.0f / 1024.0f);
    float q = 0.f;
#pragma unroll
    for (int i = 0; i < 16; ++i) { float d = x[i] - mean; q = fmaf(d, d, q); }
    const float var = wave_sum(q) * (1.0f / 1024.0f);
    const float rstd = __builtin_amdgcn_rsqf(var + 1e-5f);
    float x1[16];
#pragma unroll
    for (int i = 0; i < 16; ++i) {
      const int hf = i >> 3, e = i & 7;
      u32 sa = mq[0][hf][e >> 1], ca = mq[1][hf][e >> 1], ga = mq[2][hf][e >> 1];
      float saf = (e & 1) ? bf_hi(sa) : bf_lo(sa);
      float caf = (e & 1) ? bf_hi(ca) : bf_lo(ca);
      float gaf = (e & 1) ? bf_hi(ga) : bf_lo(ga);
      u32 yv = yq[hf][e >> 2][r * 2 + ((e >> 1) & 1)];
      float yf = (e & 1) ? bf_hi(yv) : bf_lo(yv);
      float h = fmaf((x[i] - mean) * rstd, 1.0f + caf, saf);
      x1[i] = fmaf(gaf, h * yf, x[i]);
    }
    u32x4 xo[2];
#pragma unroll
    for (int i = 0; i < 8; ++i) {
      u32 p = pk_bf(x1[2 * i], x1[2 * i + 1]);
      xo[i >> 2][i & 3] = p;
      x1[2 * i] = bf_lo(p); x1[2 * i + 1] = bf_hi(p);
    }
#pragma unroll
    for (int hf = 0; hf < 2; ++hf)
      *(u32x4*)(x1out + (size_t)(row0 + r) * D_ + hf * 512 + lane * 8) = xo[hf];
    float s2 = 0.f;
#pragma unroll
    for (int i = 0; i < 16; ++i) s2 += x1[i];
    const float mean2 = wave_sum(s2) * (1.0f / 1024.0f);
    float q2 = 0.f;
#pragma unroll
    for (int i = 0; i < 16; ++i) { float d = x1[i] - mean2; q2 = fmaf(d, d, q2); }
    const float var2 = wave_sum(q2) * (1.0f / 1024.0f);
    const float rstd2 = __builtin_amdgcn_rsqf(var2 + 1e-5f);
    u32x4 ho[2];
#pragma unroll
    for (int i = 0; i < 8; ++i) {
      const int hf = i >> 2, e = i & 3;
      u32 sb = mq[3][hf][e], cb = mq[4][hf][e];
      float h0 = fmaf((x1[2 * i] - mean2) * rstd2, 1.0f + bf_lo(cb), bf_lo(sb));
      float h1 = fmaf((x1[2 * i + 1] - mean2) * rstd2, 1.0f + bf_hi(cb), bf_hi(sb));
      ho[hf][e] = pk_bf(h0, h1);
    }
    const int row = row0 + r, mt = row >> 4, mr = row & 15;
#pragma unroll
    for (int hf = 0; hf < 2; ++hf)
      *(u32x4*)((char*)h2out + (size_t)mt * 32768 + (hf * 64 + lane) * 256 + mr * 16) = ho[hf];
    STAMP(2 + r);
  }
  STAMP_END(1);
}

constexpr int G1_R = 5;
constexpr int G1_WOFF = 16384;
constexpr int G1_UP = 7168 + 16;
constexpr int G1_SLOT = 16384 + G1_UP + 7168;
constexpr int G1_NK = 32;

struct G1Frags { bf16x8 x[4]; bf16x8 wg[3], wu[3], wm; };

__global__ __launch_bounds__(512, 2) void k2_gemm1(
    const u16* __restrict__ h2, const u16* __restrict__ w1, const u16* __restrict__ w3,
    u16* __restrict__ hh, const u16* __restrict__ w2pf) {
  extern __shared__ __attribute__((aligned(16))) char smem[];
  const int t = threadIdx.x, lane = t & 63;
  const int w = __builtin_amdgcn_readfirstlane(t >> 6);
  const int b = blockIdx.x, xcd = b & 7, idx = b >> 3;
  STAMP_DECL
  const int mblk = (xcd & 1) * 4 + (idx & 3), nblk = (xcd >> 1) * 8 + (idx >> 2);
  const int wm = w & 3, wn = w >> 2;
  const u32 lbase = (u32)(uintptr_t)(LDSP char*)smem;

  const char* xsrc = (const char*)h2 + (size_t)(mblk * 16 + 2 * w) * 32768;
  const char* wsrc = (const char*)((w >> 2) ? w3 : w1) + nblk * 224;
  const u32 xv0 = lane * 16, xv1 = lane * 16 + 32768;
  u32 wv0, wv1;
  {
    int c0 = lane, c1 = lane + 64;
    if (c1 > 111) c1 = 111;
    int p0 = 8 * (w & 3) + c0 / 14, p1 = 8 * (w & 3) + c1 / 14;
    int k0 = 8 * ((p0 >> 2) & 3) + 4 * (p0 >> 4) + (p0 & 3);
    int k1 = 8 * ((p1 >> 2) & 3) + 4 * (p1 >> 4) + (p1 & 3);
    wv0 = k0 * 7168 + (c0 % 14) * 16;
    wv1 = k1 * 7168 + (c1 % 14) * 16;
  }
  const u32 xdst = lbase + 2 * w * 1024;
  const u32 wdst = lbase + G1_WOFF + (w >> 2) * G1_UP + (w & 3) * 1792;
  auto issue = [&](int kb, u32 boff) {
    const char* xs = xsrc + kb * 1024;
    const char* ws = wsrc + (size_t)kb * (32 * 7168);
    glds16(xs, xv0, xdst + boff);
    glds16(xs, xv1, xdst + boff + 1024);
    glds16(ws, wv0, wdst + boff);
    glds16_48(ws, wv1, wdst + boff + 1024);
  };
  const int g = lane >> 4, q = (lane >> 2) & 3, p = lane & 3;
  const u32 xr = lbase + wm * 4096 + lane * 16;
  const u32 wr = lbase + G1_WOFF + (4 * g + q) * 224 + 8 * p + wn * 128;
  const u32 wrm = lbase + G1_WOFF + (4 * g + q) * 224 + (p & 1) * G1_UP + (48 + 8 * wn + 4 * (p >> 1)) * 2;
  auto rd = [&](G1Frags& f, u32 boff) {
#pragma unroll
    for (int i = 0; i < 4; ++i) f.x[i] = lds_b128(xr + boff + i * 1024);
#pragma unroll
    for (int i = 0; i < 3; ++i) {
      f.wg[i] = lds_tr2(wr + boff + 32 * i, wr + boff + 32 * i + 3584);
      f.wu[i] = lds_tr2(wr + boff + G1_UP + 32 * i, wr + boff + G1_UP + 32 * i + 3584);
    }
    f.wm = lds_tr2(wrm + boff, wrm + boff + 3584);
  };
  f32x4 ag[4][3], au[4][3], am[4];
#pragma unroll
  for (int m = 0; m < 4; ++m) {
#pragma unroll
    for (int i = 0; i < 3; ++i) { ag[m][i] = (f32x4){0.f, 0.f, 0.f, 0.f}; au[m][i] = (f32x4){0.f, 0.f, 0.f, 0.f}; }
    am[m] = (f32x4){0.f, 0.f, 0.f, 0.f};
  }
  auto mm = [&](const G1Frags& f) {
#pragma unroll
    for (int m = 0; m < 4; ++m) {
#pragma unroll
      for (int i = 0; i < 3; ++i) {
        ag[m][i] = mfma16(f.wg[i], f.x[m], ag[m][i]);
        au[m][i] = mfma16(f.wu[i], f.x[m], au[m][i]);
      }
      am[m] = mfma16(f.wm, f.x[m], am[m]);
    }
  };

  G1Frags fa, fb;
#pragma unroll
  for (int s = 0; s < G1_R; ++s) issue(s, s * G1_SLOT);
  glds4(w2pf, (u32)((b * 8 + w) * 56 + (lane < 56 ? lane : 55)) * 64u, lbase + G1_R * G1_SLOT);
  STAMP(2);
  WAIT_VM(4 * (G1_R - 1) + 1);
  BARRIER();
  STAMP(3);
  rd(fa, 0);
  u32 cur = 0;
  auto nxt = [&](u32 o) { u32 n = o + G1_SLOT; return n == G1_R * G1_SLOT ? 0u : n; };
#pragma unroll 1
  for (int tt = 0; tt < G1_NK - G1_R - 1; tt += 2) {
    u32 n1 = nxt(cur), n2 = nxt(n1);
    WAIT_VM(4 * (G1_R - 2)); BARRIER();
    issue(tt + G1_R, cur);
    rd(fb, n1);
    mm(fa);
    WAIT_VM(4 * (G1_R - 2)); BARRIER();
    issue(tt + 1 + G1_R, n1);
    rd(fa, n2);
    mm(fb);
    cur = n2;
  }
  STAMP(4);
  {
    u32 n1 = nxt(cur), n2 = nxt(n1), n3 = nxt(n2), n4 = nxt(n3);
    WAIT_VM(12); BARRIER();
    issue(G1_NK - 1, cur);
    rd(fb, n1); mm(fa);
    WAIT_VM(12); BARRIER();
    rd(fa, n2); mm(fb);
    WAIT_VM(8); BARRIER();
    rd(fb, n3); mm(fa);
    WAIT_VM(4); BARRIER();
    rd(fa, n4); mm(fb);
    WAIT_VM(0); BARRIER();
    rd(fb, cur); mm(fa);
    mm(fb);
  }

  STAMP(5);
  const int m16 = lane & 15;
  char* hbase = (char*)hh + (size_t)(mblk * 16 + wm * 4) * (112 * 1024);
  const int f0 = nblk * 112 + wn * 64;
#pragma unroll
  for (int m = 0; m < 4; ++m) {
#pragma unroll
    for (int i = 0; i < 3; ++i) {
      float o[4];
#pragma unroll
      for (int r = 0; r < 4; ++r) o[r] = silu_f(ag[m][i][r]) * au[m][i][r];
      const int f = f0 + 16 * i + 4 * g;
      *(u32x2*)(hbase + (size_t)m * (112 * 1024) + (f >> 3) * 256 + m16 * 16 + (f & 4) * 2) =
          (u32x2){pk_bf(o[0], o[1]), pk_bf(o[2], o[3])};
    }
  }
#pragma unroll
  for (int mp = 0; mp < 2; ++mp) {
    float o[4];
#pragma unroll
    for (int r = 0; r < 4; ++r) {
      auto sw = __builtin_amdgcn_permlane16_swap(__float_as_uint(am[2 * mp][r]), __float_as_uint(am[2 * mp + 1][r]), false, false);
      o[r] = silu_f(__uint_as_float(sw[0])) * __uint_as_float(sw[1]);
    }
    const int f = nblk * 112 + 48 + 8 * wn + 4 * (g >> 1);
    const int m = 2 * mp + (g & 1);
    *(u32x2*)(hbase + (size_t)m * (112 * 1024) + (f >> 3) * 256 + m16 * 16 + (f & 4) * 2) =
        (u32x2){pk_bf(o[0], o[1]), pk_bf(o[2], o[3])};
  }
  STAMP_END(2);
}

constexpr int G2_R = 6;
constexpr int G2_WOFF = 16384;
constexpr int G2_SLOT = 16384 + 8192;
constexpr int G2_NS = 56;

struct G2Frags { bf16x8 x[2]; bf16x8 w[4]; };

__global__ __launch_bounds__(512, 2) void k3_gemm2(
    const u16* __restrict__ hh, const u16* __restrict__ w2, const u16* __restrict__ x1,
    const u16* __restrict__ mod, u16* __restrict__ out) {
  extern __shared__ __attribute__((aligned(16))) char smem[];
  const int t = threadIdx.x, lane = t & 63;
  const int w = __builtin_amdgcn_readfirstlane(t >> 6);
  const int b = blockIdx.x, xcd = b & 7, idx = b >> 3;
  STAMP_DECL
  const int mblk = (xcd & 3) * 4 + (idx & 3), nblk = (xcd >> 2) * 8 + (idx >> 2);
  const int wm = w & 3, wk = w >> 2;
  const u32 lbase = (u32)(uintptr_t)(LDSP char*)smem;

  const char* xsrc = (const char*)hh + (size_t)(mblk * 8 + w) * (112 * 1024);
  const char* wsrc = (const char*)w2 + nblk * 128;
  const u32 xv0 = lane * 16, xv1 = lane * 16 + 1024;
  u32 wv;
  {
    int pos = 8 * w + (lane >> 3);
    int pp = pos & 31;
    int k = (pos & 32) + 8 * ((pp >> 2) & 3) + 4 * (pp >> 4) + (pp & 3);
    int cc = (lane & 7) ^ (((pos >> 1) & 3) << 1);
    wv = k * 2048 + cc * 16;
  }
  const u32 xdst = lbase + w * 2048;
  const u32 wdst = lbase + G2_WOFF + w * 1024;
  auto issue = [&](int s, u32 boff) {
    const char* xs = xsrc + s * 2048;
    const char* ws = wsrc + (size_t)s * (64 * 2048);
    glds16(xs, xv0, xdst + boff);
    glds16(xs, xv1, xdst + boff + 1024);
    glds16(ws, wv, wdst + boff);
  };
  const int g = lane >> 4, q = (lane >> 2) & 3, p = lane & 3;
  const u32 xr = lbase + wm * 4096 + wk * 1024 + lane * 16;
  const int key = (2 * g + (q >> 1)) & 3;
  u32 wra[4];
#pragma unroll
  for (int nt = 0; nt < 4; ++nt)
    wra[nt] = lbase + G2_WOFF + wk * 4096 + (4 * g + q) * 128 + ((nt ^ key) * 32) + 8 * p;
  auto rd = [&](G2Frags& f, u32 boff) {
    f.x[0] = lds_b128(xr + boff);
    f.x[1] = lds_b128(xr + boff + 2048);
#pragma unroll
    for (int nt = 0; nt < 4; ++nt) f.w[nt] = lds_tr2(wra[nt] + boff, wra[nt] + boff + 2048);
  };
  f32x4 acc[2][4];
#pragma unroll
  for (int m = 0; m < 2; ++m)
#pragma unroll
    for (int nt = 0; nt < 4; ++nt) acc[m][nt] = (f32x4){0.f, 0.f, 0.f, 0.f};
  auto mm = [&](const G2Frags& f) {
#pragma unroll
    for (int m = 0; m < 2; ++m)
#pragma unroll
      for (int nt = 0; nt < 4; ++nt) acc[m][nt] = mfma16(f.w[nt], f.x[m], acc[m][nt]);
  };
  G2Frags fa, fb;
#pragma unroll
  for (int s = 0; s < G2_R; ++s) issue(s, s * G2_SLOT);
  STAMP(2);
  WAIT_VM(3 * (G2_R - 1));
  BARRIER();
  STAMP(3);
  rd(fa, 0);
  u32 cur = 0;
  auto nxt = [&](u32 o) { u32 n = o + G2_SLOT; return n == G2_R * G2_SLOT ? 0u : n; };
#pragma unroll 1
  for (int tt = 0; tt < G2_NS - G2_R; tt += 2) {
    u32 n1 = nxt(cur), n2 = nxt(n1);
    WAIT_VM(3 * (G2_R - 2)); BARRIER();
    issue(tt + G2_R, cur);
    rd(fb, n1);
    mm(fa);
    WAIT_VM(3 * (G2_R - 2)); BARRIER();
    issue(tt + 1 + G2_R, n1);
    rd(fa, n2);
    mm(fb);
    cur = n2;
  }
  STAMP(4);
  const int m16 = lane & 15;
  const int row = mblk * 128 + (2 * wm + wk) * 16 + m16;
  const int col0 = nblk * 64 + 4 * g;
  {
    u32 n1 = nxt(cur), n2 = nxt(n1), n3 = nxt(n2), n4 = nxt(n3), n5 = nxt(n4);
    WAIT_VM(12); BARRIER();
    rd(fb, n1); mm(fa);
    WAIT_VM(9); BARRIER();
    rd(fa, n2); mm(fb);
    WAIT_VM(6); BARRIER();
    rd(fb, n3); mm(fa);
    WAIT_VM(3); BARRIER();
    rd(fa, n4); mm(fb);
    WAIT_VM(0); BARRIER();
    rd(fb, n5); mm(fa);
    mm(fb);
  }
  STAMP(5);
  u32x2 xq[4], gq[4];
#pragma unroll
  for (int nt = 0; nt < 4; ++nt) {
    xq[nt] = *(const u32x2*)(x1 + (size_t)row * D_ + col0 + 16 * nt);
    gq[nt] = *(const u32x2*)(mod + 5 * 1024 + col0 + 16 * nt);
  }
  BARRIER();
#pragma unroll
  for (int nt = 0; nt < 4; ++nt)
    *(LDSP f32x4*)(lbase + (w * 4 + nt) * 1024 + lane * 16) = wk ? acc[0][nt] : acc[1][nt];
  BARRIER();
#pragma unroll
  for (int nt = 0; nt < 4; ++nt) {
    f32x4 o = *(const LDSP f32x4*)(lbase + ((w ^ 4) * 4 + nt) * 1024 + lane * 16);
    f32x4 mine = wk ? acc[1][nt] : acc[0][nt];
    o += mine;
    u32 b0 = pk_bf(o[0], o[1]), b1 = pk_bf(o[2], o[3]);
    float r0 = fmaf(bf_lo(gq[nt][0]), bf_lo(b0), bf_lo(xq[nt][0]));
    float r1 = fmaf(bf_hi(gq[nt][0]), bf_hi(b0), bf_hi(xq[nt][0]));
    float r2 = fmaf(bf_lo(gq[nt][1]), bf_lo(b1), bf_lo(xq[nt][1]));
    float r3 = fmaf(bf_hi(gq[nt][1]), bf_hi(b1), bf_hi(xq[nt][1]));
    *(u32x2*)(out + (size_t)row * D_ + col0 + 16 * nt) = (u32x2){pk_bf(r0, r1), pk_bf(r2, r3)};
  }
  STAMP_END(3);
}

extern "C" void kernel_launch(void* const* d_in, const int* in_sizes, int n_in,
                              void* d_out, int out_size, void* d_ws, size_t ws_size,
                              hipStream_t stream) {
  const u16* x = (const u16*)d_in[0];
  const u16* kin = (const u16*)d_in[1];
  const u16* vin = (const u16*)d_in[2];
  const u16* shk = (const u16*)d_in[3];
  const u16* ssmk = (const u16*)d_in[4];
  const u16* ssmD = (const u16*)d_in[5];
  const u16* w1 = (const u16*)d_in[7];
  const u16* w3 = (const u16*)d_in[8];
  const u16* w2 = (const u16*)d_in[9];
  const u16* cvec = (const u16*)d_in[10];
  const u16* wmod = (const u16*)d_in[11];
  char* ws = (char*)d_ws;
  u16* mod = (u16*)(ws + WS_MOD);
  u16* yp = (u16*)(ws + WS_YP);
  u16* x1 = (u16*)(ws + WS_X1);
  u16* h2 = (u16*)(ws + WS_H2);
  u16* hh = (u16*)(ws + WS_HH);
  (void)hipFuncSetAttribute((const void*)k0_conv, hipFuncAttributeMaxDynamicSharedMemorySize, C_LDS);
  (void)hipFuncSetAttribute((const void*)k2_gemm1, hipFuncAttributeMaxDynamicSharedMemorySize, G1_R * G1_SLOT + 256);
  (void)hipFuncSetAttribute((const void*)k3_gemm2, hipFuncAttributeMaxDynamicSharedMemorySize, G2_R * G2_SLOT);
  k0_conv<<<256, 512, C_LDS, stream>>>(kin, vin, shk, ssmk, ssmD, cvec, wmod, mod, yp);
  k1_norm<<<256, 256, 0, stream>>>(x, yp, mod, x1, h2);
  k2_gemm1<<<256, 512, G1_R * G1_SLOT + 256, stream>>>(h2, w1, w3, hh, w2);
  k3_gemm2<<<256, 512, G2_R * G2_SLOT, stream>>>(hh, w2, x1, mod, (u16*)d_out);
}
```

```cpp
#include <hip/hip_runtime.h>
#include <stdint.h>

typedef unsigned short u16;
typedef unsigned int u32;
typedef short s16x4 __attribute__((ext_vector_type(4)));
typedef short s16x8 __attribute__((ext_vector_type(8)));
typedef __bf16 bf16x2 __attribute__((ext_vector_type(2)));
typedef __bf16 bf16x8 __attribute__((ext_vector_type(8)));
typedef float f32x2 __attribute__((ext_vector_type(2)));
typedef float f32x4 __attribute__((ext_vector_type(4)));
typedef u32 u32x2 __attribute__((ext_vector_type(2)));
typedef u32 u32x4 __attribute__((ext_vector_type(4)));
#define LDSP __attribute__((address_space(3)))

constexpr int S_ = 2048, D_ = 1024, F_ = 3584;

constexpr size_t WS_MOD = 0;
constexpr size_t WS_YP = 1u << 20;
constexpr size_t WS_X1 = WS_YP + (4u << 20);
constexpr size_t WS_H2 = WS_X1 + (4u << 20);
constexpr size_t WS_HH = WS_H2 + (4u << 20);

__device__ __forceinline__ float bf_lo(u32 v) { return __uint_as_float(v << 16); }
__device__ __forceinline__ float bf_hi(u32 v) { return __uint_as_float(v & 0xffff0000u); }
__device__ __forceinline__ u32 pk_bf(float lo, float hi) {
  f32x2 f = {lo, hi};
  bf16x2 b = __builtin_convertvector(f, bf16x2);
  return __builtin_bit_cast(u32, b);
}
__device__ __forceinline__ float rbf(float x) { return bf_lo(pk_bf(x, x)); }
__device__ __forceinline__ float silu_f(float x) {
  return x * __builtin_amdgcn_rcpf(1.0f + __builtin_amdgcn_exp2f(x * -1.44269504088896341f));
}
__device__ __forceinline__ f32x4 mfma16(bf16x8 a, bf16x8 b, f32x4 c) {
  return __builtin_amdgcn_mfma_f32_16x16x32_bf16(a, b, c, 0, 0, 0);
}
__device__ __forceinline__ void glds16(const void* sbase, u32 voff, u32 ldsoff) {
  asm volatile("s_mov_b32 m0, %2\n\ts_nop 0\n\tglobal_load_lds_dwordx4 %0, %1"
               :: "v"(voff), "s"(sbase), "s"(ldsoff) : "memory");
}
__device__ __forceinline__ void glds16_48(const void* sbase, u32 voff, u32 ldsoff) {
  asm volatile("s_mov_b32 m0, %2\n\ts_mov_b32 exec_hi, 0xffff\n\tglobal_load_lds_dwordx4 %0, %1\n\ts_mov_b32 exec_hi, -1"
               :: "v"(voff), "s"(sbase), "s"(ldsoff) : "memory");
}
__device__ __forceinline__ void glds4(const void* sbase, u32 voff, u32 ldsoff) {
  asm volatile("s_mov_b32 m0, %2\n\ts_nop 0\n\tglobal_load_lds_dword %0, %1"
               :: "v"(voff), "s"(sbase), "s"(ldsoff) : "memory");
}
__device__ __forceinline__ void st16(void* p, u32x4 v) {
  asm volatile("global_store_dwordx4 %0, %1, off sc1\n\ts_nop 1" :: "v"(p), "v"(v) : "memory");
}
__device__ __forceinline__ void st8(void* p, u32x2 v) {
  asm volatile("global_store_dwordx2 %0, %1, off sc1\n\ts_nop 1" :: "v"(p), "v"(v) : "memory");
}
#define SCHED0() __builtin_amdgcn_sched_barrier(0)
#define WAIT_VM(n) do { SCHED0(); asm volatile("s_waitcnt vmcnt(%0)" :: "n"(n) : "memory"); } while (0)
#define BARRIER() do { asm volatile("s_waitcnt lgkmcnt(0)\n\ts_barrier" ::: "memory"); SCHED0(); } while (0)

#define STAMP_DECL
#define STAMP(i)
#define STAMP_END(kid)

__device__ __forceinline__ bf16x8 lds_b128(u32 off) {
  return *(const LDSP bf16x8*)(off);
}
__device__ __forceinline__ bf16x8 lds_tr2(u32 off0, u32 off1) {
  s16x4 a = __builtin_amdgcn_ds_read_tr16_b64_v4i16((LDSP s16x4*)(off0));
  s16x4 b = __builtin_amdgcn_ds_read_tr16_b64_v4i16((LDSP s16x4*)(off1));
  s16x8 ab = __builtin_shufflevector(a, b, 0, 1, 2, 3, 4, 5, 6, 7);
  return __builtin_bit_cast(bf16x8, ab);
}

constexpr int C_KPAD = 448;
constexpr int C_KE = C_KPAD;
constexpr int C_KO = 4576 + C_KPAD;
constexpr int C_R1 = 9152;
constexpr int C_R0 = C_R1 + 4224;
constexpr int C_CH = 9152 + 8384;
constexpr int C_Y = 4 * C_CH;
constexpr int C_GP = C_Y + 16384;
constexpr int C_FL = C_GP + 4096;
constexpr int C_LDS = C_FL + 64;
#define K0_LA 2
#define K0_GSTAGE 1

template <typename T> __device__ __forceinline__ T ldnt(const void* p) {
  return __builtin_nontemporal_load((const T*)p);
}
__device__ __forceinline__ void lds_inc(u32 addr) {
  asm volatile("ds_add_u32 %0, %1" :: "v"(addr), "v"(1u) : "memory");
}
__device__ __forceinline__ void lds_wait(u32 addr, u32 val) {
  while (true) {
    u32 v = *(volatile const LDSP u32*)(addr);
    if ((u32)__builtin_amdgcn_readfirstlane(v) >= val) break;
    __builtin_amdgcn_s_sleep(1);
  }
  asm volatile("" ::: "memory");
}
__device__ __forceinline__ f32x2 unpk(u32 v) { return (f32x2){bf_lo(v), bf_hi(v)}; }
__device__ __forceinline__ u32 shr1(u32 old, u32 v) {
  return __builtin_amdgcn_update_dpp(old, v, 0x138, 0xf, 0xf, false);
}

template <int J>
__device__ __forceinline__ void conv_stage(f32x4 (&acc)[8], u32 la, u32 lb) {
  constexpr int NA = 8 - 2 * J;
  bf16x8 A[2][NA], B[2][2];
  auto ld = [&](int bu, u32 la_, u32 lb_) {
#pragma unroll
    for (int e = 0; e < 2; ++e) {
      u32x2 lo = *(volatile const LDSP u32x2*)(lb_ + 512 * (7 - 2 * J - e));
      u32x2 hi = *(volatile const LDSP u32x2*)(lb_ + 512 * (7 - 2 * J - e) + 8);
      u32x4 v = {lo[0], lo[1], hi[0], hi[1]};
      B[bu][e] = __builtin_bit_cast(bf16x8, v);
    }
#pragma unroll
    for (int a = 0; a < NA; ++a) A[bu][a] = lds_b128(la_ + 512 * a);
  };
  auto mm = [&](int bu) {
#pragma unroll
    for (int a = 0; a < NA; ++a) acc[a + 2 * J] = mfma16(A[bu][a], B[bu][0], acc[a + 2 * J]);
#pragma unroll
    for (int a = 0; a < NA - 1; ++a) acc[a + 2 * J + 1] = mfma16(A[bu][a], B[bu][1], acc[a + 2 * J + 1]);
  };
  auto il = [&]() {
#pragma unroll
    for (int i = 0; i < NA + 4; ++i) {
      __builtin_amdgcn_sched_group_barrier(0x008, 1, 0);
      __builtin_amdgcn_sched_group_barrier(0x100, (2 * NA - 1 >= NA + 4) ? 1 : 2, 0);
    }
    __builtin_amdgcn_sched_group_barrier(0x008, 2 * NA, 0);
    SCHED0();
  };
  ld(0, la, lb);
  SCHED0();
#pragma unroll 1
  for (int qp = 0; qp < 3; ++qp) {
    ld(1, la - 64, lb - 64);
    mm(0);
    il();
    ld(0, la - 128, lb - 128);
    mm(1);
    il();
    la -= 128; lb -= 128;
  }
  ld(1, la - 64, lb - 64);
  mm(0);
  il();
  mm(1);
}

__global__ __launch_bounds__(512, 2) void k0_conv(
    const u16* __restrict__ Kin, const u16* __restrict__ Vin, const u16* __restrict__ shk,
    const u16* __restrict__ ssmk, const u16* __restrict__ ssmD, const u16* __restrict__ cvec,
    const u16* __restrict__ wmod, float* __restrict__ modp, u16* __restrict__ yp,
    const u16* __restrict__ Xpf) {
  extern __shared__ __attribute__((aligned(16))) char smem[];
  STAMP_DECL
  const int t = threadIdx.x, lane = t & 63;
  const int w = __builtin_amdgcn_readfirstlane(t >> 6);
  const int b = blockIdx.x, xcd = b & 7, idx = b >> 3;
  const int grp = xcd * 32 + idx;
  const int d0 = grp * 4;
  const u32 lbase = (u32)(uintptr_t)(LDSP char*)smem;

  if (w >= 4) {
    const int lw = w - 4;
    const size_t ro = (size_t)(128 * lw + 2 * lane) * D_ + d0;
    const u16* kp = Kin + ro;
    const u16* vp = Vin + ro;
    u32x2 L[16];
    auto ld = [&](int n) {
      const u16* src = ((n & 2) ? vp : kp) + (size_t)(n >> 2) * 512 * D_ + (n & 1) * D_;
      L[n] = ldnt<u32x2>(src);
    };
    u32x2 hk[4][3];
#pragma unroll
    for (int j = 0; j < 4; ++j)
#pragma unroll
      for (int r = 0; r < 3; ++r) {
        const u32 row = (j == 0 && lw == 0) ? 0u : (u32)(512 * j + 128 * lw - 3 + r);
        hk[j][r] = *(const u32x2*)(Kin + (size_t)row * D_ + d0);
      }
    const u32x4 sk01 = *(const u32x4*)(shk + d0 * 4);
    const u32x4 sk23 = *(const u32x4*)(shk + d0 * 4 + 8);
#pragma unroll
    for (int n = 0; n < 4 + K0_LA; ++n) ld(n);
    SCHED0();
    __builtin_amdgcn_s_barrier();
    SCHED0();
#pragma unroll
    for (int r = 0; r < 3; ++r) {
      asm volatile("" : "+s"(hk[0][r]));
      if (lw == 0) hk[0][r] = (u32x2){0u, 0u};
    }
    f32x2 W[2][4];
#pragma unroll
    for (int hp = 0; hp < 2; ++hp) {
      const u32x4 sk = hp ? sk23 : sk01;
      W[hp][0] = (f32x2){bf_lo(sk[0]), bf_lo(sk[2])};
      W[hp][1] = (f32x2){bf_hi(sk[0]), bf_hi(sk[2])};
      W[hp][2] = (f32x2){bf_lo(sk[1]), bf_lo(sk[3])};
      W[hp][3] = (f32x2){bf_hi(sk[1]), bf_hi(sk[3])};
    }
    const u32 uw = lbase + C_R1 + (2046 - 128 * lw - 2 * lane) * 2;
#pragma unroll
    for (int j = 0; j < 4; ++j) {
#pragma unroll
      for (int hp = 0; hp < 2; ++hp) {
        const u32 kA = L[4 * j][hp], kB = L[4 * j + 1][hp];
        const u32 kB1 = shr1(hk[j][2][hp], kB);
        const u32 kA1 = shr1(hk[j][1][hp], kA);
        const u32 kB2 = shr1(hk[j][0][hp], kB1);
        const f32x2 fA = unpk(kA), fB = unpk(kB), fB1 = unpk(kB1), fA1 = unpk(kA1), fB2 = unpk(kB2);
        f32x2 a0 = fB2 * W[hp][3], a1 = fA1 * W[hp][3];
        a0 = __builtin_elementwise_fma(fA1, W[hp][2], a0); a1 = __builtin_elementwise_fma(fB1, W[hp][2], a1);
        a0 = __builtin_elementwise_fma(fB1, W[hp][1], a0); a1 = __builtin_elementwise_fma(fA, W[hp][1], a1);
        a0 = __builtin_elementwise_fma(fA, W[hp][0], a0);  a1 = __builtin_elementwise_fma(fB, W[hp][0], a1);
        a0 *= unpk(L[4 * j + 2][hp]);
        a1 *= unpk(L[4 * j + 3][hp]);
#pragma unroll
        for (int c = 0; c < 2; ++c) {
          const u32 pk = pk_bf(a1[c], a0[c]);
          const u32 o = uw + (2 * hp + c) * C_CH - j * 1024;
          *(LDSP u32*)(o) = pk;
          *(LDSP u32*)(o + (C_R0 - C_R1) - 4) = pk;
        }
      }
      if (lane == 0) lds_inc(lbase + C_FL + 4 * j);
      SCHED0();
      if (j < 3) STAMP(2 + j);
#pragma unroll
      for (int n = 4 * j + 4 + K0_LA; n < 4 * j + 8 + K0_LA; ++n) if (n < 16) ld(n);
      SCHED0();
    }
    STAMP(5);
    u32 pf = *(const u32*)(Xpf + (size_t)b * 8192 + (lw * 64 + lane) * 32);
    asm volatile("" :: "v"(pf));
  } else {
    u32 pf = ldnt<u32>(((lane & 2) ? Vin : Kin) + (size_t)(512 * w + 16 * idx + (lane >> 2)) * D_ + xcd * 128 + (lane & 1) * 64);
    const u16* fk = ssmk + (size_t)(d0 + w) * S_;
    u32x4 fc[4]; u32 fp[4];
#pragma unroll
    for (int i = 0; i < 4; ++i) {
      const int z = lane + 64 * i;
      fc[i] = ldnt<u32x4>(fk + z * 8);
      fp[i] = ldnt<u32>(fk + (z ? z : 1) * 8 - 2);
    }
    SCHED0();
    const bool hasg = idx < 24;
    const int unit = xcd * 24 + idx;
    u32x4 wq[16], cq[2];
    {
      const int row0 = (unit & 1) * 512 + w * 128 + (lane >> 3) * 16;
      cq[0] = *(const u32x4*)(cvec + row0);
      cq[1] = *(const u32x4*)(cvec + row0 + 8);
      const u32 vo = hasg ? (u32)(row0 * 6144 + (unit >> 1) * 64 + (lane & 7) * 8) * 2u : 0u;
      const u32 rs = hasg ? 12288u : 0u;
#pragma unroll
      for (int i = 0; i < 16; ++i) wq[i] = ldnt<u32x4>((const char*)wmod + (size_t)(i * rs) + vo);
    }
    const u32x2 dD = *(const u32x2*)(ssmD + d0);
    SCHED0();
    const u32 cb = lbase + w * C_CH;
    if (lane < 56) *(LDSP u32x4*)(cb + (lane / 28) * 4576 + (lane % 28) * 16) = (u32x4){0u, 0u, 0u, 0u};
    if (lane < 16) *(LDSP u32x2*)(cb + ((lane >> 3) ? C_R0 : C_R1) + 4096 + (lane & 7) * 8) = (u32x2){0u, 0u};
    if (lane == 16) *(LDSP u32*)(cb + C_R0 + 4092) = 0u;
    if (t < 16) *(LDSP u32*)(lbase + C_FL + 4 * t) = 0u;
    BARRIER();
    const u32 dsel = (w & 2) ? dD[1] : dD[0];
    const float Dv = (w & 1) ? bf_hi(dsel) : bf_lo(dsel);
#pragma unroll
    for (int i = 0; i < 4; ++i) {
      const int z = lane + 64 * i;
      u32 prev = z ? fp[i] : 0u;
      u32x4 e;
      e[0] = __builtin_amdgcn_alignbit(fc[i][0], prev, 16);
      e[1] = __builtin_amdgcn_alignbit(fc[i][1], fc[i][0], 16);
      e[2] = __builtin_amdgcn_alignbit(fc[i][2], fc[i][1], 16);
      e[3] = __builtin_amdgcn_alignbit(fc[i][3], fc[i][2], 16);
      *(LDSP u32x4*)(cb + C_KE + z * 16) = e;
      *(LDSP u32x4*)(cb + C_KO + z * 16) = fc[i];
    }
    asm volatile("" :: "v"(pf));
    const int g = lane >> 4, i16 = lane & 15;
    const u32 la = cb + ((i16 & 1) ? C_KO : C_KE) + 64 * (i16 >> 1) + 16 * g;
    const u32 lb = cb + ((i16 & 1) ? C_R1 : C_R0) + 4088 - 8 * (i16 >> 1) + 16 * g - 3584;
    f32x4 acc[8];
#pragma unroll
    for (int c = 0; c < 8; ++c) acc[c] = (f32x4){0.f, 0.f, 0.f, 0.f};
    const u32 ur = cb + C_R1 + (2046 - 64 * g - 2 * i16) * 2;
    const u32 yw = lbase + C_Y + w * 4096 + (64 * g + 2 * i16) * 2;
    const u32 yr = lbase + C_Y + (128 * w + 2 * lane) * 2;
    u16* ydst = yp + ((size_t)(8 * w + (lane >> 3)) * 256 + grp) * 64 + (lane & 7) * 8;
    auto fin = [&](int j) {
#pragma unroll
      for (int c = 2 * j; c < 2 * j + 2; ++c) {
        const u32 u01 = *(const LDSP u32*)(ur - 512 * c);
        const u32 u23 = *(const LDSP u32*)(ur - 512 * c - 64);
        *(LDSP u32*)(yw + 512 * c) = pk_bf(fmaf(bf_hi(u01), Dv, acc[c][0]), fmaf(bf_lo(u01), Dv, acc[c][1]));
        *(LDSP u32*)(yw + 512 * c + 64) = pk_bf(fmaf(bf_hi(u23), Dv, acc[c][2]), fmaf(bf_lo(u23), Dv, acc[c][3]));
      }
      if (lane == 0) lds_inc(lbase + C_FL + 16 + 4 * j);
      lds_wait(lbase + C_FL + 16 + 4 * j, 4);
      u32 y[4];
#pragma unroll
      for (int ch = 0; ch < 4; ++ch) y[ch] = *(const LDSP u32*)(yr + ch * 4096 + j * 1024);
      u32x4 o = {__builtin_amdgcn_perm(y[1], y[0], 0x05040100u), __builtin_amdgcn_perm(y[3], y[2], 0x05040100u),
                 __builtin_amdgcn_perm(y[1], y[0], 0x07060302u), __builtin_amdgcn_perm(y[3], y[2], 0x07060302u)};
      st16(ydst + (size_t)j * (32 * 256 * 64), o);
    };
    auto gemv = [&]() {
      if (!hasg) return;
      u32 sc[8];
#pragma unroll
      for (int i = 0; i < 8; ++i) {
        u32 cv = cq[i >> 2][i & 3];
        sc[i] = pk_bf(silu_f(bf_lo(cv)), silu_f(bf_hi(cv)));
      }
      float ga[8];
#pragma unroll
      for (int j = 0; j < 8; ++j) ga[j] = 0.f;
#pragma unroll
      for (int i = 0; i < 8; ++i) {
#pragma unroll
        for (int j = 0; j < 4; ++j) {
          u32 a = wq[2 * i][j], bb = wq[2 * i + 1][j];
          u32 lo = __builtin_amdgcn_perm(bb, a, 0x05040100u);
          u32 hi = __builtin_amdgcn_perm(bb, a, 0x07060302u);
          ga[2 * j] = __builtin_amdgcn_fdot2_f32_bf16(__builtin_bit_cast(bf16x2, lo), __builtin_bit_cast(bf16x2, sc[i]), ga[2 * j], false);
          ga[2 * j + 1] = __builtin_amdgcn_fdot2_f32_bf16(__builtin_bit_cast(bf16x2, hi), __builtin_bit_cast(bf16x2, sc[i]), ga[2 * j + 1], false);
        }
      }
#pragma unroll
      for (int j = 0; j < 8; ++j)
        ga[j] += __uint_as_float(__builtin_amdgcn_update_dpp(0u, __float_as_uint(ga[j]), 0x118, 0xf, 0xf, true));
      if (lane & 8) {
        u32 o = lbase + C_GP + ((w * 4 + (lane >> 4)) * 64 + (lane & 7) * 8) * 4;
        *(LDSP f32x4*)(o) = (f32x4){ga[0], ga[1], ga[2], ga[3]};
        *(LDSP f32x4*)(o + 16) = (f32x4){ga[4], ga[5], ga[6], ga[7]};
      }
      u32 old = 0;
      if (lane == 0)
        asm volatile("ds_add_rtn_u32 %0, %1, %2\n\ts_waitcnt lgkmcnt(0)" : "=v"(old) : "v"(lbase + C_FL + 32), "v"(1u) : "memory");
      if (__builtin_amdgcn_readfirstlane(old) == 3) {
        float s = 0.f;
#pragma unroll
        for (int i = 0; i < 16; ++i) s += *(const LDSP float*)(lbase + C_GP + (i * 64 + lane) * 4);
        modp[(unit & 1) * 6144 + (unit >> 1) * 64 + lane] = s;
      }
    };
    lds_wait(lbase + C_FL + 0, 4);
    STAMP(2);
    conv_stage<0>(acc, la, lb);
    if (K0_GSTAGE == 0) gemv();
    fin(0);
    STAMP(3);
    lds_wait(lbase + C_FL + 4, 4);
    conv_stage<1>(acc, la, lb);
    if (K0_GSTAGE == 1) gemv();
    fin(1);
    STAMP(4);
    lds_wait(lbase + C_FL + 8, 4);
    conv_stage<2>(acc, la, lb);
    if (K0_GSTAGE == 2) gemv();
    fin(2);
    lds_wait(lbase + C_FL + 12, 4);
    STAMP(5);
    conv_stage<3>(acc, la, lb);
    if (K0_GSTAGE == 3) gemv();
    fin(3);
  }
  STAMP_END(0);
}

__device__ __forceinline__ float wave_sum(float x) {
  x += __uint_as_float(__builtin_amdgcn_update_dpp(0u, __float_as_uint(x), 0x111, 0xf, 0xf, true));
  x += __uint_as_float(__builtin_amdgcn_update_dpp(0u, __float_as_uint(x), 0x112, 0xf, 0xf, true));
  x += __uint_as_float(__builtin_amdgcn_update_dpp(0u, __float_as_uint(x), 0x114, 0xf, 0xf, true));
  x += __uint_as_float(__builtin_amdgcn_update_dpp(0u, __float_as_uint(x), 0x118, 0xf, 0xf, true));
  x += __uint_as_float(__builtin_amdgcn_update_dpp(0u, __float_as_uint(x), 0x142, 0xa, 0xf, true));
  x += __uint_as_float(__builtin_amdgcn_update_dpp(0u, __float_as_uint(x), 0x143, 0xc, 0xf, true));
  return __uint_as_float(__builtin_amdgcn_readlane(__float_as_uint(x), 63));
}

constexpr int N_YRS = 2048 + 64;
constexpr int N_MOD = 8 * N_YRS;
constexpr int N_H = N_MOD + 10240;
constexpr int N_LDS = N_H + 16384;
#define K1_PFROWS 1

__global__ __launch_bounds__(512) void k1_norm(
    const u16* __restrict__ X, const u16* __restrict__ yp, const float* __restrict__ modp,
    u16* __restrict__ x1out, u16* __restrict__ h2out,
    const u16* __restrict__ w1pf, const u16* __restrict__ w3pf) {
  __shared__ __attribute__((aligned(16))) char smem[N_LDS];
  const int t = threadIdx.x, lane = t & 63;
  const int w = __builtin_amdgcn_readfirstlane(t >> 6);
  STAMP_DECL
  const int rb = blockIdx.x;
  const int row = rb * 8 + w;
  const u32 lbase = (u32)(uintptr_t)(LDSP char*)smem;
  u32x4 xq[2], yq[2];
  f32x4 mp[3][2];
#pragma unroll
  for (int i = 0; i < 2; ++i)
    yq[i] = *(const u32x4*)(yp + ((size_t)(rb >> 1) * 256 + (t >> 2) + 128 * i) * 64 + (rb & 1) * 32 + (t & 3) * 8);
#pragma unroll
  for (int i = 0; i < 3; ++i)
    if (i < 2 || w < 4) {
      mp[i][0] = *(const f32x4*)(modp + (t + 512 * i) * 4);
      mp[i][1] = *(const f32x4*)(modp + 6144 + (t + 512 * i) * 4);
    }
#pragma unroll
  for (int hf = 0; hf < 2; ++hf) xq[hf] = *(const u32x4*)(X + (size_t)row * D_ + hf * 512 + lane * 8);
  SCHED0();
  u32 pf = 0;
  if (w < 4 * K1_PFROWS) {
    const u16* src = ((w & 2) ? w3pf : w1pf) + (size_t)(rb + 256 * (w >> 2)) * F_ + ((w & 1) * 56 + (lane < 56 ? lane : 55)) * 32;
    pf = *(const u32*)src;
  }
  SCHED0();
#pragma unroll
  for (int i = 0; i < 2; ++i) {
    const u32 o = lbase + (t & 3) * (2 * N_YRS) + ((t >> 2) + 128 * i) * 8;
    *(LDSP u32x2*)(o) = (u32x2){yq[i][0], yq[i][1]};
    *(LDSP u32x2*)(o + N_YRS) = (u32x2){yq[i][2], yq[i][3]};
  }
#pragma unroll
  for (int i = 0; i < 3; ++i)
    if (i < 2 || w < 4) {
      f32x4 s = mp[i][0] + mp[i][1];
      *(LDSP u32x2*)(lbase + N_MOD + (t + 512 * i) * 8) = (u32x2){pk_bf(s[0], s[1]), pk_bf(s[2], s[3])};
    }
  float x[16];
#pragma unroll
  for (int i = 0; i < 8; ++i) {
    u32 v = xq[i >> 2][i & 3];
    x[2 * i] = bf_lo(v); x[2 * i + 1] = bf_hi(v);
  }
  float s = 0.f;
#pragma unroll
  for (int i = 0; i < 16; ++i) s += x[i];
  const float mean = wave_sum(s) * (1.0f / 1024.0f);
  float q = 0.f;
#pragma unroll
  for (int i = 0; i < 16; ++i) { float d = x[i] - mean; q = fmaf(d, d, q); }
  const float var = wave_sum(q) * (1.0f / 1024.0f);
  const float rstd = __builtin_amdgcn_rsqf(var + 1e-5f);
  STAMP(2);
  BARRIER();
  STAMP(3);
  u32x4 yv[2], mq[5][2];
#pragma unroll
  for (int hf = 0; hf < 2; ++hf) yv[hf] = *(const LDSP u32x4*)(lbase + w * N_YRS + hf * 1024 + lane * 16);
#pragma unroll
  for (int m = 0; m < 5; ++m)
#pragma unroll
    for (int hf = 0; hf < 2; ++hf) mq[m][hf] = *(const LDSP u32x4*)(lbase + N_MOD + m * 2048 + hf * 1024 + lane * 16);
  float x1[16];
#pragma unroll
  for (int i = 0; i < 16; ++i) {
    const int hf = i >> 3, e = i & 7;
    u32 sa = mq[0][hf][e >> 1], ca = mq[1][hf][e >> 1], ga = mq[2][hf][e >> 1], yy = yv[hf][e >> 1];
    float saf = (e & 1) ? bf_hi(sa) : bf_lo(sa);
    float caf = (e & 1) ? bf_hi(ca) : bf_lo(ca);
    float gaf = (e & 1) ? bf_hi(ga) : bf_lo(ga);
    float yf = (e & 1) ? bf_hi(yy) : bf_lo(yy);
    float h = fmaf((x[i] - mean) * rstd, 1.0f + caf, saf);
    x1[i] = fmaf(gaf, h * yf, x[i]);
  }
  u32x4 xo[2];
#pragma unroll
  for (int i = 0; i < 8; ++i) {
    u32 p = pk_bf(x1[2 * i], x1[2 * i + 1]);
    xo[i >> 2][i & 3] = p;
    x1[2 * i] = bf_lo(p); x1[2 * i + 1] = bf_hi(p);
  }
#pragma unroll
  for (int hf = 0; hf < 2; ++hf) st16(x1out + (size_t)row * D_ + hf * 512 + lane * 8, xo[hf]);
  float s2 = 0.f;
#pragma unroll
  for (int i = 0; i < 16; ++i) s2 += x1[i];
  const float mean2 = wave_sum(s2) * (1.0f / 1024.0f);
  float q2 = 0.f;
#pragma unroll
  for (int i = 0; i < 16; ++i) { float d = x1[i] - mean2; q2 = fmaf(d, d, q2); }
  const float var2 = wave_sum(q2) * (1.0f / 1024.0f);
  const float rstd2 = __builtin_amdgcn_rsqf(var2 + 1e-5f);
#pragma unroll
  for (int hf = 0; hf < 2; ++hf) {
    u32x4 ho;
#pragma unroll
    for (int e = 0; e < 4; ++e) {
      const int i = hf * 4 + e;
      u32 sb = mq[3][hf][e], cb = mq[4][hf][e];
      float h0 = fmaf((x1[2 * i] - mean2) * rstd2, 1.0f + bf_lo(cb), bf_lo(sb));
      float h1 = fmaf((x1[2 * i + 1] - mean2) * rstd2, 1.0f + bf_hi(cb), bf_hi(sb));
      ho[e] = pk_bf(h0, h1);
    }
    *(LDSP u32x4*)(lbase + N_H + (hf * 64 + lane) * 128 + ((w ^ (lane >> 1)) & 7) * 16) = ho;
  }
  STAMP(4);
  BARRIER();
#pragma unroll
  for (int i = 0; i < 2; ++i) {
    const int chunk = (t >> 3) + 64 * i, r = t & 7;
    u32x4 v = *(const LDSP u32x4*)(lbase + N_H + chunk * 128 + ((r ^ (chunk >> 1)) & 7) * 16);
    st16((char*)h2out + (size_t)(rb >> 1) * 32768 + chunk * 256 + (rb & 1) * 128 + r * 16, v);
  }
  asm volatile("" :: "v"(pf));
  STAMP_END(1);
}

constexpr int G1_R = 5;
constexpr int G1_WOFF = 16384;
constexpr int G1_UP = 7168 + 16;
constexpr int G1_SLOT = 16384 + G1_UP + 7168;
constexpr int G1_NK = 32;

struct G1Frags { bf16x8 x[4]; bf16x8 wg[3], wu[3], wm; };

__global__ __launch_bounds__(512, 2) void k2_gemm1(
    const u16* __restrict__ h2, const u16* __restrict__ w1, const u16* __restrict__ w3,
    u16* __restrict__ hh, const u16* __restrict__ w2pf) {
  extern __shared__ __attribute__((aligned(16))) char smem[];
  const int t = threadIdx.x, lane = t & 63;
  const int w = __builtin_amdgcn_readfirstlane(t >> 6);
  const int b = blockIdx.x, xcd = b & 7, idx = b >> 3;
  STAMP_DECL
  const int mblk = (xcd & 1) * 4 + (idx & 3), nblk = (xcd >> 1) * 8 + (idx >> 2);
  const int wm = w & 3, wn = w >> 2;
  const u32 lbase = (u32)(uintptr_t)(LDSP char*)smem;

  const char* xsrc = (const char*)h2 + (size_t)(mblk * 16 + 2 * w) * 32768;
  const char* wsrc = (const char*)((w >> 2) ? w3 : w1) + nblk * 224;
  const u32 xv0 = lane * 16, xv1 = lane * 16 + 32768;
  u32 wv0, wv1;
  {
    int c0 = lane, c1 = lane + 64;
    if (c1 > 111) c1 = 111;
    int p0 = 8 * (w & 3) + c0 / 14, p1 = 8 * (w & 3) + c1 / 14;
    int k0 = 8 * ((p0 >> 2) & 3) + 4 * (p0 >> 4) + (p0 & 3);
    int k1 = 8 * ((p1 >> 2) & 3) + 4 * (p1 >> 4) + (p1 & 3);
    wv0 = k0 * 7168 + (c0 % 14) * 16;
    wv1 = k1 * 7168 + (c1 % 14) * 16;
  }
  const u32 xdst = lbase + 2 * w * 1024;
  const u32 wdst = lbase + G1_WOFF + (w >> 2) * G1_UP + (w & 3) * 1792;
  auto issue = [&](int kb, u32 boff) {
    const char* xs = xsrc + kb * 1024;
    const char* ws = wsrc + (size_t)kb * (32 * 7168);
    glds16(xs, xv0, xdst + boff);
    glds16(xs, xv1, xdst + boff + 1024);
    glds16(ws, wv0, wdst + boff);
    glds16_48(ws, wv1, wdst + boff + 1024);
  };
  const int g = lane >> 4, q = (lane >> 2) & 3, p = lane & 3;
  const u32 xr = lbase + wm * 4096 + lane * 16;
  const u32 wr = lbase + G1_WOFF + (4 * g + q) * 224 + 8 * p + wn * 128;
  const u32 wrm = lbase + G1_WOFF + (4 * g + q) * 224 + (p & 1) * G1_UP + (48 + 8 * wn + 4 * (p >> 1)) * 2;
  auto rd = [&](G1Frags& f, u32 boff) {
#pragma unroll
    for (int i = 0; i < 4; ++i) f.x[i] = lds_b128(xr + boff + i * 1024);
#pragma unroll
    for (int i = 0; i < 3; ++i) {
      f.wg[i] = lds_tr2(wr + boff + 32 * i, wr + boff + 32 * i + 3584);
      f.wu[i] = lds_tr2(wr + boff + G1_UP + 32 * i, wr + boff + G1_UP + 32 * i + 3584);
    }
    f.wm = lds_tr2(wrm + boff, wrm + boff + 3584);
  };
  f32x4 ag[4][3], au[4][3], am[4];
#pragma unroll
  for (int m = 0; m < 4; ++m) {
#pragma unroll
    for (int i = 0; i < 3; ++i) { ag[m][i] = (f32x4){0.f, 0.f, 0.f, 0.f}; au[m][i] = (f32x4){0.f, 0.f, 0.f, 0.f}; }
    am[m] = (f32x4){0.f, 0.f, 0.f, 0.f};
  }
  auto mm = [&](const G1Frags& f) {
#pragma unroll
    for (int m = 0; m < 4; ++m) {
#pragma unroll
      for (int i = 0; i < 3; ++i) {
        ag[m][i] = mfma16(f.wg[i], f.x[m], ag[m][i]);
        au[m][i] = mfma16(f.wu[i], f.x[m], au[m][i]);
      }
      am[m] = mfma16(f.wm, f.x[m], am[m]);
    }
  };

  G1Frags fa, fb;
#pragma unroll
  for (int s = 0; s < G1_R; ++s) issue(s, s * G1_SLOT);
  glds4(w2pf, (u32)((b * 8 + w) * 56 + (lane < 56 ? lane : 55)) * 64u, lbase + G1_R * G1_SLOT);
  STAMP(2);
  WAIT_VM(4 * (G1_R - 1) + 1);
  BARRIER();
  STAMP(3);
  rd(fa, 0);
  u32 cur = 0;
  auto nxt = [&](u32 o) { u32 n = o + G1_SLOT; return n == G1_R * G1_SLOT ? 0u : n; };
#pragma unroll 1
  for (int tt = 0; tt < G1_NK - G1_R - 1; tt += 2) {
    u32 n1 = nxt(cur), n2 = nxt(n1);
    WAIT_VM(4 * (G1_R - 2)); BARRIER();
    issue(tt + G1_R, cur);
    rd(fb, n1);
    mm(fa);
    WAIT_VM(4 * (G1_R - 2)); BARRIER();
    issue(tt + 1 + G1_R, n1);
    rd(fa, n2);
    mm(fb);
    cur = n2;
  }
  STAMP(4);
  {
    u32 n1 = nxt(cur), n2 = nxt(n1), n3 = nxt(n2), n4 = nxt(n3);
    WAIT_VM(12); BARRIER();
    issue(G1_NK - 1, cur);
    rd(fb, n1); mm(fa);
    WAIT_VM(12); BARRIER();
    rd(fa, n2); mm(fb);
    WAIT_VM(8); BARRIER();
    rd(fb, n3); mm(fa);
    WAIT_VM(4); BARRIER();
    rd(fa, n4); mm(fb);
    WAIT_VM(0); BARRIER();
    rd(fb, cur); mm(fa);
    mm(fb);
  }

  STAMP(5);
  const int m16 = lane & 15;
  char* hbase = (char*)hh + (size_t)(mblk * 16 + wm * 4) * (112 * 1024);
  const int f0 = nblk * 112 + wn * 64;
#pragma unroll
  for (int m = 0; m < 4; ++m) {
#pragma unroll
    for (int i = 0; i < 3; ++i) {
      float o[4];
#pragma unroll
      for (int r = 0; r < 4; ++r) o[r] = silu_f(ag[m][i][r]) * au[m][i][r];
      const int f = f0 + 16 * i + 4 * g;
      st8(hbase + (size_t)m * (112 * 1024) + (f >> 3) * 256 + m16 * 16 + (f & 4) * 2,
          (u32x2){pk_bf(o[0], o[1]), pk_bf(o[2], o[3])});
    }
  }
#pragma unroll
  for (int mp = 0; mp < 2; ++mp) {
    float o[4];
#pragma unroll
    for (int r = 0; r < 4; ++r) {
      auto sw = __builtin_amdgcn_permlane16_swap(__float_as_uint(am[2 * mp][r]), __float_as_uint(am[2 * mp + 1][r]), false, false);
      o[r] = silu_f(__uint_as_float(sw[0])) * __uint_as_float(sw[1]);
    }
    const int f = nblk * 112 + 48 + 8 * wn + 4 * (g >> 1);
    const int m = 2 * mp + (g & 1);
    st8(hbase + (size_t)m * (112 * 1024) + (f >> 3) * 256 + m16 * 16 + (f & 4) * 2,
        (u32x2){pk_bf(o[0], o[1]), pk_bf(o[2], o[3])});
  }
  STAMP_END(2);
}

constexpr int G2_R = 6;
constexpr int G2_WOFF = 16384;
constexpr int G2_SLOT = 16384 + 8192;
constexpr int G2_NS = 56;

struct G2Frags { bf16x8 x[2]; bf16x8 w[4]; };

__global__ __launch_bounds__(512, 2) void k3_gemm2(
    const u16* __restrict__ hh, const u16* __restrict__ w2, const u16* __restrict__ x1,
    const float* __restrict__ modp, u16* __restrict__ out) {
  extern __shared__ __attribute__((aligned(16))) char smem[];
  const int t = threadIdx.x, lane = t & 63;
  const int w = __builtin_amdgcn_readfirstlane(t >> 6);
  const int b = blockIdx.x, xcd = b & 7, idx = b >> 3;
  STAMP_DECL
  const int mblk = (xcd & 3) * 4 + (idx & 3), nblk = (xcd >> 2) * 8 + (idx >> 2);
  const int wm = w & 3, wk = w >> 2;
  const u32 lbase = (u32)(uintptr_t)(LDSP char*)smem;

  const char* xsrc = (const char*)hh + (size_t)(mblk * 8 + w) * (112 * 1024);
  const char* wsrc = (const char*)w2 + nblk * 128;
  const u32 xv0 = lane * 16, xv1 = lane * 16 + 1024;
  u32 wv;
  {
    int pos = 8 * w + (lane >> 3);
    int pp = pos & 31;
    int k = (pos & 32) + 8 * ((pp >> 2) & 3) + 4 * (pp >> 4) + (pp & 3);
    int cc = (lane & 7) ^ (((pos >> 1) & 3) << 1);
    wv = k * 2048 + cc * 16;
  }
  const u32 xdst = lbase + w * 2048;
  const u32 wdst = lbase + G2_WOFF + w * 1024;
  auto issue = [&](int s, u32 boff) {
    const char* xs = xsrc + s * 2048;
    const char* ws = wsrc + (size_t)s * (64 * 2048);
    glds16(xs, xv0, xdst + boff);
    glds16(xs, xv1, xdst + boff + 1024);
    glds16(ws, wv, wdst + boff);
  };
  const int g = lane >> 4, q = (lane >> 2) & 3, p = lane & 3;
  const u32 xr = lbase + wm * 4096 + wk * 1024 + lane * 16;
  const int key = (2 * g + (q >> 1)) & 3;
  u32 wra[4];
#pragma unroll
  for (int nt = 0; nt < 4; ++nt)
    wra[nt] = lbase + G2_WOFF + wk * 4096 + (4 * g + q) * 128 + ((nt ^ key) * 32) + 8 * p;
  auto rd = [&](G2Frags& f, u32 boff) {
    f.x[0] = lds_b128(xr + boff);
    f.x[1] = lds_b128(xr + boff + 2048);
#pragma unroll
    for (int nt = 0; nt < 4; ++nt) f.w[nt] = lds_tr2(wra[nt] + boff, wra[nt] + boff + 2048);
  };
  f32x4 acc[2][4];
#pragma unroll
  for (int m = 0; m < 2; ++m)
#pragma unroll
    for (int nt = 0; nt < 4; ++nt) acc[m][nt] = (f32x4){0.f, 0.f, 0.f, 0.f};
  auto mm = [&](const G2Frags& f) {
#pragma unroll
    for (int m = 0; m < 2; ++m)
#pragma unroll
      for (int nt = 0; nt < 4; ++nt) acc[m][nt] = mfma16(f.w[nt], f.x[m], acc[m][nt]);
  };
  G2Frags fa, fb;
#pragma unroll
  for (int s = 0; s < G2_R; ++s) issue(s, s * G2_SLOT);
  STAMP(2);
  WAIT_VM(3 * (G2_R - 1));
  BARRIER();
  STAMP(3);
  rd(fa, 0);
  u32 cur = 0;
  auto nxt = [&](u32 o) { u32 n = o + G2_SLOT; return n == G2_R * G2_SLOT ? 0u : n; };
#pragma unroll 1
  for (int tt = 0; tt < G2_NS - G2_R; tt += 2) {
    u32 n1 = nxt(cur), n2 = nxt(n1);
    WAIT_VM(3 * (G2_R - 2)); BARRIER();
    issue(tt + G2_R, cur);
    rd(fb, n1);
    mm(fa);
    WAIT_VM(3 * (G2_R - 2)); BARRIER();
    issue(tt + 1 + G2_R, n1);
    rd(fa, n2);
    mm(fb);
    cur = n2;
  }
  STAMP(4);
  const int m16 = lane & 15;
  const int row = mblk * 128 + (2 * wm + wk) * 16 + m16;
  const int col0 = nblk * 64 + 4 * g;
  {
    u32 n1 = nxt(cur), n2 = nxt(n1), n3 = nxt(n2), n4 = nxt(n3), n5 = nxt(n4);
    WAIT_VM(12); BARRIER();
    rd(fb, n1); mm(fa);
    WAIT_VM(9); BARRIER();
    rd(fa, n2); mm(fb);
    WAIT_VM(6); BARRIER();
    rd(fb, n3); mm(fa);
    WAIT_VM(3); BARRIER();
    rd(fa, n4); mm(fb);
    WAIT_VM(0); BARRIER();
    rd(fb, n5); mm(fa);
    mm(fb);
  }
  STAMP(5);
  u32x2 xq[4], gq[4];
  f32x4 gp[4][2];
#pragma unroll
  for (int nt = 0; nt < 4; ++nt) {
    xq[nt] = *(const u32x2*)(x1 + (size_t)row * D_ + col0 + 16 * nt);
    gp[nt][0] = *(const f32x4*)(modp + 5 * 1024 + col0 + 16 * nt);
    gp[nt][1] = *(const f32x4*)(modp + 6144 + 5 * 1024 + col0 + 16 * nt);
  }
  BARRIER();
#pragma unroll
  for (int nt = 0; nt < 4; ++nt)
    *(LDSP f32x4*)(lbase + (w * 4 + nt) * 1024 + lane * 16) = wk ? acc[0][nt] : acc[1][nt];
  BARRIER();
#pragma unroll
  for (int nt = 0; nt < 4; ++nt) {
    f32x4 o = *(const LDSP f32x4*)(lbase + ((w ^ 4) * 4 + nt) * 1024 + lane * 16);
    f32x4 mine = wk ? acc[1][nt] : acc[0][nt];
    o += mine;
    { f32x4 gs = gp[nt][0] + gp[nt][1]; gq[nt] = (u32x2){pk_bf(gs[0], gs[1]), pk_bf(gs[2], gs[3])}; }
    u32 b0 = pk_bf(o[0], o[1]), b1 = pk_bf(o[2], o[3]);
    float r0 = fmaf(bf_lo(gq[nt][0]), bf_lo(b0), bf_lo(xq[nt][0]));
    float r1 = fmaf(bf_hi(gq[nt][0]), bf_hi(b0), bf_hi(xq[nt][0]));
    float r2 = fmaf(bf_lo(gq[nt][1]), bf_lo(b1), bf_lo(xq[nt][1]));
    float r3 = fmaf(bf_hi(gq[nt][1]), bf_hi(b1), bf_hi(xq[nt][1]));
    *(u32x2*)(out + (size_t)row * D_ + col0 + 16 * nt) = (u32x2){pk_bf(r0, r1), pk_bf(r2, r3)};
  }
  STAMP_END(3);
}

extern "C" void kernel_launch(void* const* d_in, const int* in_sizes, int n_in,
                              void* d_out, int out_size, void* d_ws, size_t ws_size,
                              hipStream_t stream) {
  const u16* x = (const u16*)d_in[0];
  const u16* kin = (const u16*)d_in[1];
  const u16* vin = (const u16*)d_in[2];
  const u16* shk = (const u16*)d_in[3];
  const u16* ssmk = (const u16*)d_in[4];
  const u16* ssmD = (const u16*)d_in[5];
  const u16* w1 = (const u16*)d_in[7];
  const u16* w3 = (const u16*)d_in[8];
  const u16* w2 = (const u16*)d_in[9];
  const u16* cvec = (const u16*)d_in[10];
  const u16* wmod = (const u16*)d_in[11];
  char* ws = (char*)d_ws;
  float* mod = (float*)(ws + WS_MOD);
  u16* yp = (u16*)(ws + WS_YP);
  u16* x1 = (u16*)(ws + WS_X1);
  u16* h2 = (u16*)(ws + WS_H2);
  u16* hh = (u16*)(ws + WS_HH);
  (void)hipFuncSetAttribute((const void*)k0_conv, hipFuncAttributeMaxDynamicSharedMemorySize, C_LDS);
  (void)hipFuncSetAttribute((const void*)k2_gemm1, hipFuncAttributeMaxDynamicSharedMemorySize, G1_R * G1_SLOT + 256);
  (void)hipFuncSetAttribute((const void*)k3_gemm2, hipFuncAttributeMaxDynamicSharedMemorySize, G2_R * G2_SLOT);
  k0_conv<<<256, 512, C_LDS, stream>>>(kin, vin, shk, ssmk, ssmD, cvec, wmod, mod, yp, x);
  k1_norm<<<256, 512, 0, stream>>>(x, yp, mod, x1, h2, w1, w3);
  k2_gemm1<<<256, 512, G1_R * G1_SLOT + 256, stream>>>(h2, w1, w3, hh, w2);
  k3_gemm2<<<256, 512, G2_R * G2_SLOT, stream>>>(hh, w2, x1, mod, (u16*)d_out);
}
```

```cpp
#include <hip/hip_runtime.h>
#include <stdint.h>

typedef unsigned short u16;
typedef unsigned int u32;
typedef short s16x4 __attribute__((ext_vector_type(4)));
typedef short s16x8 __attribute__((ext_vector_type(8)));
typedef __bf16 bf16x2 __attribute__((ext_vector_type(2)));
typedef __bf16 bf16x8 __attribute__((ext_vector_type(8)));
typedef float f32x2 __attribute__((ext_vector_type(2)));
typedef float f32x4 __attribute__((ext_vector_type(4)));
typedef u32 u32x2 __attribute__((ext_vector_type(2)));
typedef u32 u32x4 __attribute__((ext_vector_type(4)));
#define LDSP __attribute__((address_space(3)))

constexpr int S_ = 2048, D_ = 1024, F_ = 3584;

constexpr size_t WS_MOD = 0;
constexpr size_t WS_YP = 1u << 20;
constexpr size_t WS_X1 = WS_YP + (4u << 20);
constexpr size_t WS_H2 = WS_X1 + (4u << 20);
constexpr size_t WS_HH = WS_H2 + (4u << 20);

__device__ __forceinline__ float bf_lo(u32 v) { return __uint_as_float(v << 16); }
__device__ __forceinline__ float bf_hi(u32 v) { return __uint_as_float(v & 0xffff0000u); }
__device__ __forceinline__ u32 pk_bf(float lo, float hi) {
  f32x2 f = {lo, hi};
  bf16x2 b = __builtin_convertvector(f, bf16x2);
  return __builtin_bit_cast(u32, b);
}
__device__ __forceinline__ float rbf(float x) { return bf_lo(pk_bf(x, x)); }
__device__ __forceinline__ float silu_f(float x) {
  return x * __builtin_amdgcn_rcpf(1.0f + __builtin_amdgcn_exp2f(x * -1.44269504088896341f));
}
__device__ __forceinline__ f32x4 mfma16(bf16x8 a, bf16x8 b, f32x4 c) {
  return __builtin_amdgcn_mfma_f32_16x16x32_bf16(a, b, c, 0, 0, 0);
}
__device__ __forceinline__ void glds16(const void* sbase, u32 voff, u32 ldsoff) {
  asm volatile("s_mov_b32 m0, %2\n\ts_nop 0\n\tglobal_load_lds_dwordx4 %0, %1"
               :: "v"(voff), "s"(sbase), "s"(ldsoff) : "memory");
}
__device__ __forceinline__ void glds16_48(const void* sbase, u32 voff, u32 ldsoff) {
  asm volatile("s_mov_b32 m0, %2\n\ts_mov_b32 exec_hi, 0xffff\n\tglobal_load_lds_dwordx4 %0, %1\n\ts_mov_b32 exec_hi, -1"
               :: "v"(voff), "s"(sbase), "s"(ldsoff) : "memory");
}
__device__ __forceinline__ void glds4(const void* sbase, u32 voff, u32 ldsoff) {
  asm volatile("s_mov_b32 m0, %2\n\ts_nop 0\n\tglobal_load_lds_dword %0, %1"
               :: "v"(voff), "s"(sbase), "s"(ldsoff) : "memory");
}
__device__ __forceinline__ void st16(void* p, u32x4 v) {
  asm volatile("global_store_dwordx4 %0, %1, off sc1\n\ts_nop 1" :: "v"(p), "v"(v) : "memory");
}
__device__ __forceinline__ void st8(void* p, u32x2 v) {
  asm volatile("global_store_dwordx2 %0, %1, off sc1\n\ts_nop 1" :: "v"(p), "v"(v) : "memory");
}
#define SCHED0() __builtin_amdgcn_sched_barrier(0)
#define WAIT_VM(n) do { SCHED0(); asm volatile("s_waitcnt vmcnt(%0)" :: "n"(n) : "memory"); } while (0)
#define BARRIER() do { __builtin_amdgcn_s_waitcnt(0xc07f); asm volatile("s_barrier" ::: "memory"); SCHED0(); } while (0)

#define STAMP_DECL
#define STAMP(i)
#define STAMP_END(kid)

__device__ __forceinline__ bf16x8 lds_b128(u32 off) {
  return *(const LDSP bf16x8*)(off);
}
__device__ __forceinline__ bf16x8 lds_tr2(u32 off0, u32 off1) {
  s16x4 a = __builtin_amdgcn_ds_read_tr16_b64_v4i16((LDSP s16x4*)(off0));
  s16x4 b = __builtin_amdgcn_ds_read_tr16_b64_v4i16((LDSP s16x4*)(off1));
  s16x8 ab = __builtin_shufflevector(a, b, 0, 1, 2, 3, 4, 5, 6, 7);
  return __builtin_bit_cast(bf16x8, ab);
}

constexpr int C_KPAD = 448;
constexpr int C_KE = C_KPAD;
constexpr int C_KO = 4576 + C_KPAD;
constexpr int C_R1 = 9152;
constexpr int C_R0 = C_R1 + 4224;
constexpr int C_CH = 9152 + 8384;
constexpr int C_Y = 4 * C_CH;
constexpr int C_GP = C_Y + 16384;
constexpr int C_FL = C_GP + 4096;
constexpr int C_LDS = C_FL + 64;
#define K0_LA 2
#define K0_GSTAGE 1

template <typename T> __device__ __forceinline__ T ldnt(const void* p) {
  return __builtin_nontemporal_load((const T*)p);
}
__device__ __forceinline__ void lds_inc(u32 addr) {
  asm volatile("ds_add_u32 %0, %1" :: "v"(addr), "v"(1u) : "memory");
}
__device__ __forceinline__ void lds_wait(u32 addr, u32 val) {
  while (true) {
    u32 v = *(volatile const LDSP u32*)(addr);
    if ((u32)__builtin_amdgcn_readfirstlane(v) >= val) break;
    __builtin_amdgcn_s_sleep(1);
  }
  asm volatile("" ::: "memory");
}
__device__ __forceinline__ f32x2 unpk(u32 v) { return (f32x2){bf_lo(v), bf_hi(v)}; }
__device__ __forceinline__ u32 shr1(u32 old, u32 v) {
  return __builtin_amdgcn_update_dpp(old, v, 0x138, 0xf, 0xf, false);
}

template <int J>
__device__ __forceinline__ void conv_stage(f32x4 (&acc)[8], u32 la, u32 lb) {
  constexpr int NA = 8 - 2 * J;
  bf16x8 A[2][NA], B[2][2];
  auto ld = [&](int bu, u32 la_, u32 lb_) {
#pragma unroll
    for (int e = 0; e < 2; ++e) {
      u32x2 lo = *(volatile const LDSP u32x2*)(lb_ + 512 * (7 - 2 * J - e));
      u32x2 hi = *(volatile const LDSP u32x2*)(lb_ + 512 * (7 - 2 * J - e) + 8);
      u32x4 v = {lo[0], lo[1], hi[0], hi[1]};
      B[bu][e] = __builtin_bit_cast(bf16x8, v);
    }
#pragma unroll
    for (int a = 0; a < NA; ++a) A[bu][a] = lds_b128(la_ + 512 * a);
  };
  auto mm = [&](int bu) {
#pragma unroll
    for (int a = 0; a < NA; ++a) acc[a + 2 * J] = mfma16(A[bu][a], B[bu][0], acc[a + 2 * J]);
#pragma unroll
    for (int a = 0; a < NA - 1; ++a) acc[a + 2 * J + 1] = mfma16(A[bu][a], B[bu][1], acc[a + 2 * J + 1]);
  };
  auto il = [&]() {
#pragma unroll
    for (int i = 0; i < NA + 4; ++i) {
      __builtin_amdgcn_sched_group_barrier(0x008, 1, 0);
      __builtin_amdgcn_sched_group_barrier(0x100, (2 * NA - 1 >= NA + 4) ? 1 : 2, 0);
    }
    __builtin_amdgcn_sched_group_barrier(0x008, 2 * NA, 0);
    SCHED0();
  };
  ld(0, la, lb);
  SCHED0();
#pragma unroll 1
  for (int qp = 0; qp < 3; ++qp) {
    ld(1, la - 64, lb - 64);
    mm(0);
    il();
    ld(0, la - 128, lb - 128);
    mm(1);
    il();
    la -= 128; lb -= 128;
  }
  ld(1, la - 64, lb - 64);
  mm(0);
  il();
  mm(1);
}

__global__ __launch_bounds__(512, 2) void k0_conv(
    const u16* __restrict__ Kin, const u16* __restrict__ Vin, const u16* __restrict__ shk,
    const u16* __restrict__ ssmk, const u16* __restrict__ ssmD, const u16* __restrict__ cvec,
    const u16* __restrict__ wmod, float* __restrict__ modp, u16* __restrict__ yp,
    const u16* __restrict__ Xpf) {
  extern __shared__ __attribute__((aligned(16))) char smem[];
  STAMP_DECL
  const int t = threadIdx.x, lane = t & 63;
  const int w = __builtin_amdgcn_readfirstlane(t >> 6);
  const int b = blockIdx.x, xcd = b & 7, idx = b >> 3;
  const int grp = xcd * 32 + idx;
  const int d0 = grp * 4;
  const u32 lbase = (u32)(uintptr_t)(LDSP char*)smem;

  if (w >= 4) {
    const int lw = w - 4;
    const size_t ro = (size_t)(128 * lw + 2 * lane) * D_ + d0;
    const u16* kp = Kin + ro;
    const u16* vp = Vin + ro;
    u32x2 L[16];
    auto ld = [&](int n) {
      const u16* src = ((n & 2) ? vp : kp) + (size_t)(n >> 2) * 512 * D_ + (n & 1) * D_;
      L[n] = ldnt<u32x2>(src);
    };
    u32x2 hk[4][3];
#pragma unroll
    for (int j = 0; j < 4; ++j)
#pragma unroll
      for (int r = 0; r < 3; ++r) {
        const u32 row = (j == 0 && lw == 0) ? 0u : (u32)(512 * j + 128 * lw - 3 + r);
        hk[j][r] = *(const u32x2*)(Kin + (size_t)row * D_ + d0);
      }
    const u32x4 sk01 = *(const u32x4*)(shk + d0 * 4);
    const u32x4 sk23 = *(const u32x4*)(shk + d0 * 4 + 8);
#pragma unroll
    for (int n = 0; n < 4 + K0_LA; ++n) ld(n);
    SCHED0();
    __builtin_amdgcn_s_barrier();
    SCHED0();
#pragma unroll
    for (int r = 0; r < 3; ++r) {
      asm volatile("" : "+s"(hk[0][r]));
      if (lw == 0) hk[0][r] = (u32x2){0u, 0u};
    }
    f32x2 W[2][4];
#pragma unroll
    for (int hp = 0; hp < 2; ++hp) {
      const u32x4 sk = hp ? sk23 : sk01;
      W[hp][0] = (f32x2){bf_lo(sk[0]), bf_lo(sk[2])};
      W[hp][1] = (f32x2){bf_hi(sk[0]), bf_hi(sk[2])};
      W[hp][2] = (f32x2){bf_lo(sk[1]), bf_lo(sk[3])};
      W[hp][3] = (f32x2){bf_hi(sk[1]), bf_hi(sk[3])};
    }
    const u32 uw = lbase + C_R1 + (2046 - 128 * lw - 2 * lane) * 2;
#pragma unroll
    for (int j = 0; j < 4; ++j) {
#pragma unroll
      for (int hp = 0; hp < 2; ++hp) {
        const u32 kA = L[4 * j][hp], kB = L[4 * j + 1][hp];
        const u32 kB1 = shr1(hk[j][2][hp], kB);
        const u32 kA1 = shr1(hk[j][1][hp], kA);
        const u32 kB2 = shr1(hk[j][0][hp], kB1);
        const f32x2 fA = unpk(kA), fB = unpk(kB), fB1 = unpk(kB1), fA1 = unpk(kA1), fB2 = unpk(kB2);
        f32x2 a0 = fB2 * W[hp][3], a1 = fA1 * W[hp][3];
        a0 = __builtin_elementwise_fma(fA1, W[hp][2], a0); a1 = __builtin_elementwise_fma(fB1, W[hp][2], a1);
        a0 = __builtin_elementwise_fma(fB1, W[hp][1], a0); a1 = __builtin_elementwise_fma(fA, W[hp][1], a1);
        a0 = __builtin_elementwise_fma(fA, W[hp][0], a0);  a1 = __builtin_elementwise_fma(fB, W[hp][0], a1);
        a0 *= unpk(L[4 * j + 2][hp]);
        a1 *= unpk(L[4 * j + 3][hp]);
#pragma unroll
        for (int c = 0; c < 2; ++c) {
          const u32 pk = pk_bf(a1[c], a0[c]);
          const u32 o = uw + (2 * hp + c) * C_CH - j * 1024;
          *(LDSP u32*)(o) = pk;
          *(LDSP u32*)(o + (C_R0 - C_R1) - 4) = pk;
        }
      }
      if (lane == 0) lds_inc(lbase + C_FL + 4 * j);
      SCHED0();
      if (j < 3) STAMP(2 + j);
#pragma unroll
      for (int n = 4 * j + 4 + K0_LA; n < 4 * j + 8 + K0_LA; ++n) if (n < 16) ld(n);
      SCHED0();
    }
    STAMP(5);
    u32 pf = *(const u32*)(Xpf + (size_t)b * 8192 + (lw * 64 + lane) * 32);
    asm volatile("" :: "v"(pf));
  } else {
    u32 pf = ldnt<u32>(((lane & 2) ? Vin : Kin) + (size_t)(512 * w + 16 * idx + (lane >> 2)) * D_ + xcd * 128 + (lane & 1) * 64);
    const u16* fk = ssmk + (size_t)(d0 + w) * S_;
    u32x4 fc[4]; u32 fp[4];
#pragma unroll
    for (int i = 0; i < 4; ++i) {
      const int z = lane + 64 * i;
      fc[i] = ldnt<u32x4>(fk + z * 8);
      fp[i] = ldnt<u32>(fk + (z ? z : 1) * 8 - 2);
    }
    SCHED0();
    const bool hasg = idx < 24;
    const int unit = xcd * 24 + idx;
    u32x4 wq[16], cq[2];
    {
      const int row0 = (unit & 1) * 512 + w * 128 + (lane >> 3) * 16;
      cq[0] = *(const u32x4*)(cvec + row0);
      cq[1] = *(const u32x4*)(cvec + row0 + 8);
      const u32 vo = hasg ? (u32)(row0 * 6144 + (unit >> 1) * 64 + (lane & 7) * 8) * 2u : 0u;
      const u32 rs = hasg ? 12288u : 0u;
#pragma unroll
      for (int i = 0; i < 16; ++i) wq[i] = ldnt<u32x4>((const char*)wmod + (size_t)(i * rs) + vo);
    }
    const u32x2 dD = *(const u32x2*)(ssmD + d0);
    SCHED0();
    const u32 cb = lbase + w * C_CH;
    if (lane < 56) *(LDSP u32x4*)(cb + (lane / 28) * 4576 + (lane % 28) * 16) = (u32x4){0u, 0u, 0u, 0u};
    if (lane < 16) *(LDSP u32x2*)(cb + ((lane >> 3) ? C_R0 : C_R1) + 4096 + (lane & 7) * 8) = (u32x2){0u, 0u};
    if (lane == 16) *(LDSP u32*)(cb + C_R0 + 4092) = 0u;
    if (t < 16) *(LDSP u32*)(lbase + C_FL + 4 * t) = 0u;
    BARRIER();
    const u32 dsel = (w & 2) ? dD[1] : dD[0];
    const float Dv = (w & 1) ? bf_hi(dsel) : bf_lo(dsel);
#pragma unroll
    for (int i = 0; i < 4; ++i) {
      const int z = lane + 64 * i;
      u32 prev = z ? fp[i] : 0u;
      u32x4 e;
      e[0] = __builtin_amdgcn_alignbit(fc[i][0], prev, 16);
      e[1] = __builtin_amdgcn_alignbit(fc[i][1], fc[i][0], 16);
      e[2] = __builtin_amdgcn_alignbit(fc[i][2], fc[i][1], 16);
      e[3] = __builtin_amdgcn_alignbit(fc[i][3], fc[i][2], 16);
      *(LDSP u32x4*)(cb + C_KE + z * 16) = e;
      *(LDSP u32x4*)(cb + C_KO + z * 16) = fc[i];
    }
    asm volatile("" :: "v"(pf));
    const int g = lane >> 4, i16 = lane & 15;
    const u32 la = cb + ((i16 & 1) ? C_KO : C_KE) + 64 * (i16 >> 1) + 16 * g;
    const u32 lb = cb + ((i16 & 1) ? C_R1 : C_R0) + 4088 - 8 * (i16 >> 1) + 16 * g - 3584;
    f32x4 acc[8];
#pragma unroll
    for (int c = 0; c < 8; ++c) acc[c] = (f32x4){0.f, 0.f, 0.f, 0.f};
    const u32 ur = cb + C_R1 + (2046 - 64 * g - 2 * i16) * 2;
    const u32 yw = lbase + C_Y + w * 4096 + (64 * g + 2 * i16) * 2;
    const u32 yr = lbase + C_Y + (128 * w + 2 * lane) * 2;
    u16* ydst = yp + ((size_t)(8 * w + (lane >> 3)) * 256 + grp) * 64 + (lane & 7) * 8;
    auto fin = [&](int j) {
#pragma unroll
      for (int c = 2 * j; c < 2 * j + 2; ++c) {
        const u32 u01 = *(const LDSP u32*)(ur - 512 * c);
        const u32 u23 = *(const LDSP u32*)(ur - 512 * c - 64);
        *(LDSP u32*)(yw + 512 * c) = pk_bf(fmaf(bf_hi(u01), Dv, acc[c][0]), fmaf(bf_lo(u01), Dv, acc[c][1]));
        *(LDSP u32*)(yw + 512 * c + 64) = pk_bf(fmaf(bf_hi(u23), Dv, acc[c][2]), fmaf(bf_lo(u23), Dv, acc[c][3]));
      }
      if (lane == 0) lds_inc(lbase + C_FL + 16 + 4 * j);
      lds_wait(lbase + C_FL + 16 + 4 * j, 4);
      u32 y[4];
#pragma unroll
      for (int ch = 0; ch < 4; ++ch) y[ch] = *(const LDSP u32*)(yr + ch * 4096 + j * 1024);
      u32x4 o = {__builtin_amdgcn_perm(y[1], y[0], 0x05040100u), __builtin_amdgcn_perm(y[3], y[2], 0x05040100u),
                 __builtin_amdgcn_perm(y[1], y[0], 0x07060302u), __builtin_amdgcn_perm(y[3], y[2], 0x07060302u)};
      st16(ydst + (size_t)j * (32 * 256 * 64), o);
    };
    auto gemv = [&]() {
      if (!hasg) return;
      u32 sc[8];
#pragma unroll
      for (int i = 0; i < 8; ++i) {
        u32 cv = cq[i >> 2][i & 3];
        sc[i] = pk_bf(silu_f(bf_lo(cv)), silu_f(bf_hi(cv)));
      }
      float ga[8];
#pragma unroll
      for (int j = 0; j < 8; ++j) ga[j] = 0.f;
#pragma unroll
      for (int i = 0; i < 8; ++i) {
#pragma unroll
        for (int j = 0; j < 4; ++j) {
          u32 a = wq[2 * i][j], bb = wq[2 * i + 1][j];
          u32 lo = __builtin_amdgcn_perm(bb, a, 0x05040100u);
          u32 hi = __builtin_amdgcn_perm(bb, a, 0x07060302u);
          ga[2 * j] = __builtin_amdgcn_fdot2_f32_bf16(__builtin_bit_cast(bf16x2, lo), __builtin_bit_cast(bf16x2, sc[i]), ga[2 * j], false);
          ga[2 * j + 1] = __builtin_amdgcn_fdot2_f32_bf16(__builtin_bit_cast(bf16x2, hi), __builtin_bit_cast(bf16x2, sc[i]), ga[2 * j + 1], false);
        }
      }
#pragma unroll
      for (int j = 0; j < 8; ++j)
        ga[j] += __uint_as_float(__builtin_amdgcn_update_dpp(0u, __float_as_uint(ga[j]), 0x118, 0xf, 0xf, true));
      if (lane & 8) {
        u32 o = lbase + C_GP + ((w * 4 + (lane >> 4)) * 64 + (lane & 7) * 8) * 4;
        *(LDSP f32x4*)(o) = (f32x4){ga[0], ga[1], ga[2], ga[3]};
        *(LDSP f32x4*)(o + 16) = (f32x4){ga[4], ga[5], ga[6], ga[7]};
      }
      u32 old = 0;
      if (lane == 0)
        asm volatile("ds_add_rtn_u32 %0, %1, %2\n\ts_waitcnt lgkmcnt(0)" : "=v"(old) : "v"(lbase + C_FL + 32), "v"(1u) : "memory");
      if (__builtin_amdgcn_readfirstlane(old) == 3) {
        float s = 0.f;
#pragma unroll
        for (int i = 0; i < 16; ++i) s += *(const LDSP float*)(lbase + C_GP + (i * 64 + lane) * 4);
        modp[(unit & 1) * 6144 + (unit >> 1) * 64 + lane] = s;
      }
    };
    lds_wait(lbase + C_FL + 0, 4);
    STAMP(2);
    conv_stage<0>(acc, la, lb);
    if (K0_GSTAGE == 0) gemv();
    fin(0);
    STAMP(3);
    lds_wait(lbase + C_FL + 4, 4);
    conv_stage<1>(acc, la, lb);
    if (K0_GSTAGE == 1) gemv();
    fin(1);
    STAMP(4);
    lds_wait(lbase + C_FL + 8, 4);
    conv_stage<2>(acc, la, lb);
    if (K0_GSTAGE == 2) gemv();
    fin(2);
    lds_wait(lbase + C_FL + 12, 4);
    STAMP(5);
    conv_stage<3>(acc, la, lb);
    if (K0_GSTAGE == 3) gemv();
    fin(3);
  }
  STAMP_END(0);
}

__device__ __forceinline__ float wave_sum(float x) {
  x += __uint_as_float(__builtin_amdgcn_update_dpp(0u, __float_as_uint(x), 0x111, 0xf, 0xf, true));
  x += __uint_as_float(__builtin_amdgcn_update_dpp(0u, __float_as_uint(x), 0x112, 0xf, 0xf, true));
  x += __uint_as_float(__builtin_amdgcn_update_dpp(0u, __float_as_uint(x), 0x114, 0xf, 0xf, true));
  x += __uint_as_float(__builtin_amdgcn_update_dpp(0u, __float_as_uint(x), 0x118, 0xf, 0xf, true));
  x += __uint_as_float(__builtin_amdgcn_update_dpp(0u, __float_as_uint(x), 0x142, 0xa, 0xf, true));
  x += __uint_as_float(__builtin_amdgcn_update_dpp(0u, __float_as_uint(x), 0x143, 0xc, 0xf, true));
  return __uint_as_float(__builtin_amdgcn_readlane(__float_as_uint(x), 63));
}

constexpr int N_YRS = 2048 + 64;
constexpr int N_MOD = 8 * N_YRS;
constexpr int N_H = N_MOD + 10240;
constexpr int N_LDS = N_H + 16384;
#define K1_PFROWS 1

__global__ __launch_bounds__(512) void k1_norm(
    const u16* __restrict__ X, const u16* __restrict__ yp, const float* __restrict__ modp,
    u16* __restrict__ x1out, u16* __restrict__ h2out,
    const u16* __restrict__ w1pf, const u16* __restrict__ w3pf) {
  __shared__ __attribute__((aligned(16))) char smem[N_LDS];
  const int t = threadIdx.x, lane = t & 63;
  const int w = __builtin_amdgcn_readfirstlane(t >> 6);
  STAMP_DECL
  const int rb = blockIdx.x;
  const int row = rb * 8 + w;
  const u32 lbase = (u32)(uintptr_t)(LDSP char*)smem;
  u32x4 xq[2], yq[2];
  f32x4 mp[3][2];
#pragma unroll
  for (int i = 0; i < 2; ++i)
    yq[i] = *(const u32x4*)(yp + ((size_t)(rb >> 1) * 256 + (t >> 2) + 128 * i) * 64 + (rb & 1) * 32 + (t & 3) * 8);
#pragma unroll
  for (int i = 0; i < 3; ++i)
    if (i < 2 || w < 4) {
      mp[i][0] = *(const f32x4*)(modp + (t + 512 * i) * 4);
      mp[i][1] = *(const f32x4*)(modp + 6144 + (t + 512 * i) * 4);
    }
#pragma unroll
  for (int hf = 0; hf < 2; ++hf) xq[hf] = *(const u32x4*)(X + (size_t)row * D_ + hf * 512 + lane * 8);
  SCHED0();
  u32 pf = 0;
  if (w < 4 * K1_PFROWS) {
    const u16* src = ((w & 2) ? w3pf : w1pf) + (size_t)(rb + 256 * (w >> 2)) * F_ + ((w & 1) * 56 + (lane < 56 ? lane : 55)) * 32;
    pf = *(const u32*)src;
  }
  SCHED0();
#pragma unroll
  for (int i = 0; i < 2; ++i) {
    const u32 o = lbase + (t & 3) * (2 * N_YRS) + ((t >> 2) + 128 * i) * 8;
    *(LDSP u32x2*)(o) = (u32x2){yq[i][0], yq[i][1]};
    *(LDSP u32x2*)(o + N_YRS) = (u32x2){yq[i][2], yq[i][3]};
  }
#pragma unroll
  for (int i = 0; i < 3; ++i)
    if (i < 2 || w < 4) {
      f32x4 s = mp[i][0] + mp[i][1];
      *(LDSP u32x2*)(lbase + N_MOD + (t + 512 * i) * 8) = (u32x2){pk_bf(s[0], s[1]), pk_bf(s[2], s[3])};
    }
  float x[16];
#pragma unroll
  for (int i = 0; i < 8; ++i) {
    u32 v = xq[i >> 2][i & 3];
    x[2 * i] = bf_lo(v); x[2 * i + 1] = bf_hi(v);
  }
  float s = 0.f;
#pragma unroll
  for (int i = 0; i < 16; ++i) s += x[i];
  const float mean = wave_sum(s) * (1.0f / 1024.0f);
  float q = 0.f;
#pragma unroll
  for (int i = 0; i < 16; ++i) { float d = x[i] - mean; q = fmaf(d, d, q); }
  const float var = wave_sum(q) * (1.0f / 1024.0f);
  const float rstd = __builtin_amdgcn_rsqf(var + 1e-5f);
  STAMP(2);
  BARRIER();
  STAMP(3);
  u32x4 yv[2], mq[5][2];
#pragma unroll
  for (int hf = 0; hf < 2; ++hf) yv[hf] = *(const LDSP u32x4*)(lbase + w * N_YRS + hf * 1024 + lane * 16);
#pragma unroll
  for (int m = 0; m < 5; ++m)
#pragma unroll
    for (int hf = 0; hf < 2; ++hf) mq[m][hf] = *(const LDSP u32x4*)(lbase + N_MOD + m * 2048 + hf * 1024 + lane * 16);
  float x1[16];
#pragma unroll
  for (int i = 0; i < 16; ++i) {
    const int hf = i >> 3, e = i & 7;
    u32 sa = mq[0][hf][e >> 1], ca = mq[1][hf][e >> 1], ga = mq[2][hf][e >> 1], yy = yv[hf][e >> 1];
    float saf = (e & 1) ? bf_hi(sa) : bf_lo(sa);
    float caf = (e & 1) ? bf_hi(ca) : bf_lo(ca);
    float gaf = (e & 1) ? bf_hi(ga) : bf_lo(ga);
    float yf = (e & 1) ? bf_hi(yy) : bf_lo(yy);
    float h = fmaf((x[i] - mean) * rstd, 1.0f + caf, saf);
    x1[i] = fmaf(gaf, h * yf, x[i]);
  }
  u32x4 xo[2];
#pragma unroll
  for (int i = 0; i < 8; ++i) {
    u32 p = pk_bf(x1[2 * i], x1[2 * i + 1]);
    xo[i >> 2][i & 3] = p;
    x1[2 * i] = bf_lo(p); x1[2 * i + 1] = bf_hi(p);
  }
#pragma unroll
  for (int hf = 0; hf < 2; ++hf) st16(x1out + (size_t)row * D_ + hf * 512 + lane * 8, xo[hf]);
  float s2 = 0.f;
#pragma unroll
  for (int i = 0; i < 16; ++i) s2 += x1[i];
  const float mean2 = wave_sum(s2) * (1.0f / 1024.0f);
  float q2 = 0.f;
#pragma unroll
  for (int i = 0; i < 16; ++i) { float d = x1[i] - mean2; q2 = fmaf(d, d, q2); }
  const float var2 = wave_sum(q2) * (1.0f / 1024.0f);
  const float rstd2 = __builtin_amdgcn_rsqf(var2 + 1e-5f);
#pragma unroll
  for (int hf = 0; hf < 2; ++hf) {
    u32x4 ho;
#pragma unroll
    for (int e = 0; e < 4; ++e) {
      const int i = hf * 4 + e;
      u32 sb = mq[3][hf][e], cb = mq[4][hf][e];
      float h0 = fmaf((x1[2 * i] - mean2) * rstd2, 1.0f + bf_lo(cb), bf_lo(sb));
      float h1 = fmaf((x1[2 * i + 1] - mean2) * rstd2, 1.0f + bf_hi(cb), bf_hi(sb));
      ho[e] = pk_bf(h0, h1);
    }
    *(LDSP u32x4*)(lbase + N_H + (hf * 64 + lane) * 128 + ((w ^ (lane >> 1)) & 7) * 16) = ho;
  }
  STAMP(4);
  BARRIER();
#pragma unroll
  for (int i = 0; i < 2; ++i) {
    const int chunk = (t >> 3) + 64 * i, r = t & 7;
    u32x4 v = *(const LDSP u32x4*)(lbase + N_H + chunk * 128 + ((r ^ (chunk >> 1)) & 7) * 16);
    st16((char*)h2out + (size_t)(rb >> 1) * 32768 + chunk * 256 + (rb & 1) * 128 + r * 16, v);
  }
  asm volatile("" :: "v"(pf));
  STAMP_END(1);
}

constexpr int G1_R = 5;
constexpr int G1_WOFF = 16384;
constexpr int G1_UP = 7168 + 16;
constexpr int G1_SLOT = 16384 + G1_UP + 7168;
constexpr int G1_NK = 32;

struct G1Frags { bf16x8 x[4]; bf16x8 wg[3], wu[3], wm; };

__global__ __launch_bounds__(512, 2) void k2_gemm1(
    const u16* __restrict__ h2, const u16* __restrict__ w1, const u16* __restrict__ w3,
    u16* __restrict__ hh, const u16* __restrict__ w2pf) {
  extern __shared__ __attribute__((aligned(16))) char smem[];
  const int t = threadIdx.x, lane = t & 63;
  const int w = __builtin_amdgcn_readfirstlane(t >> 6);
  const int b = blockIdx.x, xcd = b & 7, idx = b >> 3;
  STAMP_DECL
  const int mblk = (xcd & 1) * 4 + (idx & 3), nblk = (xcd >> 1) * 8 + (idx >> 2);
  const int wm = w & 3, wn = w >> 2;
  const u32 lbase = (u32)(uintptr_t)(LDSP char*)smem;

  const char* xsrc = (const char*)h2 + (size_t)(mblk * 16 + 2 * w) * 32768;
  const char* wsrc = (const char*)((w >> 2) ? w3 : w1) + nblk * 224;
  const u32 xv0 = lane * 16, xv1 = lane * 16 + 32768;
  u32 wv0, wv1;
  {
    int c0 = lane, c1 = lane + 64;
    if (c1 > 111) c1 = 111;
    int p0 = 8 * (w & 3) + c0 / 14, p1 = 8 * (w & 3) + c1 / 14;
    int k0 = 8 * ((p0 >> 2) & 3) + 4 * (p0 >> 4) + (p0 & 3);
    int k1 = 8 * ((p1 >> 2) & 3) + 4 * (p1 >> 4) + (p1 & 3);
    wv0 = k0 * 7168 + (c0 % 14) * 16;
    wv1 = k1 * 7168 + (c1 % 14) * 16;
  }
  const u32 xdst = lbase + 2 * w * 1024;
  const u32 wdst = lbase + G1_WOFF + (w >> 2) * G1_UP + (w & 3) * 1792;
  auto issue = [&](int kb, u32 boff) {
    const char* xs = xsrc + kb * 1024;
    const char* ws = wsrc + (size_t)kb * (32 * 7168);
    glds16(xs, xv0, xdst + boff);
    glds16(xs, xv1, xdst + boff + 1024);
    glds16(ws, wv0, wdst + boff);
    glds16_48(ws, wv1, wdst + boff + 1024);
  };
  const int g = lane >> 4, q = (lane >> 2) & 3, p = lane & 3;
  const u32 xr = lbase + wm * 4096 + lane * 16;
  const u32 wr = lbase + G1_WOFF + (4 * g + q) * 224 + 8 * p + wn * 128;
  const u32 wrm = lbase + G1_WOFF + (4 * g + q) * 224 + (p & 1) * G1_UP + (48 + 8 * wn + 4 * (p >> 1)) * 2;
  auto rd = [&](G1Frags& f, u32 boff) {
#pragma unroll
    for (int i = 0; i < 4; ++i) f.x[i] = lds_b128(xr + boff + i * 1024);
#pragma unroll
    for (int i = 0; i < 3; ++i) {
      f.wg[i] = lds_tr2(wr + boff + 32 * i, wr + boff + 32 * i + 3584);
      f.wu[i] = lds_tr2(wr + boff + G1_UP + 32 * i, wr + boff + G1_UP + 32 * i + 3584);
    }
    f.wm = lds_tr2(wrm + boff, wrm + boff + 3584);
  };
  f32x4 ag[4][3], au[4][3], am[4];
#pragma unroll
  for (int m = 0; m < 4; ++m) {
#pragma unroll
    for (int i = 0; i < 3; ++i) { ag[m][i] = (f32x4){0.f, 0.f, 0.f, 0.f}; au[m][i] = (f32x4){0.f, 0.f, 0.f, 0.f}; }
    am[m] = (f32x4){0.f, 0.f, 0.f, 0.f};
  }
  auto mm = [&](const G1Frags& f) {
#pragma unroll
    for (int m = 0; m < 4; ++m) {
#pragma unroll
      for (int i = 0; i < 3; ++i) {
        ag[m][i] = mfma16(f.wg[i], f.x[m], ag[m][i]);
        au[m][i] = mfma16(f.wu[i], f.x[m], au[m][i]);
      }
      am[m] = mfma16(f.wm, f.x[m], am[m]);
    }
  };

  G1Frags fa, fb;
  issue(0, 0);
  SCHED0();
  __builtin_amdgcn_s_barrier();
  SCHED0();
#pragma unroll
  for (int s = 1; s < G1_R; ++s) issue(s, s * G1_SLOT);
  glds4(w2pf, (u32)((b * 8 + w) * 56 + (lane < 56 ? lane : 55)) * 64u, lbase + G1_R * G1_SLOT);
  STAMP(2);
  WAIT_VM(4 * (G1_R - 1) + 1);
  BARRIER();
  STAMP(3);
  rd(fa, 0);
  if (wn) __builtin_amdgcn_s_setprio(1);
  u32 cur = 0;
  auto nxt = [&](u32 o) { u32 n = o + G1_SLOT; return n == G1_R * G1_SLOT ? 0u : n; };
  auto il = [&]() {
#pragma unroll
    for (int i = 0; i < 18; ++i) {
      __builtin_amdgcn_sched_group_barrier(0x008, 1, 0);
      __builtin_amdgcn_sched_group_barrier(0x100, 1, 0);
    }
    __builtin_amdgcn_sched_group_barrier(0x008, 10, 0);
    SCHED0();
  };
  auto step = [&](G1Frags& fc, G1Frags& fn, int kb, u32 bc, u32 bn) {
    if (!wn) issue(kb, bc);
    SCHED0();
    rd(fn, bn);
    mm(fc);
    il();
    if (wn) issue(kb, bc);
    SCHED0();
  };
  auto step0 = [&](G1Frags& fc, G1Frags& fn, u32 bn) {
    rd(fn, bn);
    mm(fc);
    il();
  };
#pragma unroll 1
  for (int tt = 0; tt < G1_NK - G1_R - 1; tt += 2) {
    u32 n1 = nxt(cur), n2 = nxt(n1);
    WAIT_VM(4 * (G1_R - 2)); BARRIER();
    step(fa, fb, tt + G1_R, cur, n1);
    WAIT_VM(4 * (G1_R - 2)); BARRIER();
    step(fb, fa, tt + 1 + G1_R, n1, n2);
    cur = n2;
  }
  STAMP(4);
  {
    u32 n1 = nxt(cur), n2 = nxt(n1), n3 = nxt(n2), n4 = nxt(n3);
    WAIT_VM(12); BARRIER();
    step(fa, fb, G1_NK - 1, cur, n1);
    WAIT_VM(12); BARRIER();
    step0(fb, fa, n2);
    WAIT_VM(8); BARRIER();
    step0(fa, fb, n3);
    WAIT_VM(4); BARRIER();
    step0(fb, fa, n4);
    WAIT_VM(0); BARRIER();
    step0(fa, fb, cur);
    mm(fb);
  }

  STAMP(5);
  const int m16 = lane & 15;
  char* hbase = (char*)hh + (size_t)(mblk * 16 + wm * 4) * (112 * 1024) + m16 * 16;
  const int c0 = nblk * 14 + wn * 8;
  auto swiglu4 = [&](const f32x4& gt, const f32x4& up) {
    float o[4];
#pragma unroll
    for (int r = 0; r < 4; ++r) o[r] = silu_f(gt[r]) * up[r];
    return (u32x2){pk_bf(o[0], o[1]), pk_bf(o[2], o[3])};
  };
#pragma unroll
  for (int mp = 0; mp < 2; ++mp) {
#pragma unroll
    for (int i = 0; i < 3; ++i) {
      u32x2 pa = swiglu4(ag[2 * mp][i], au[2 * mp][i]);
      u32x2 pb = swiglu4(ag[2 * mp + 1][i], au[2 * mp + 1][i]);
      auto s0 = __builtin_amdgcn_permlane16_swap(pa[0], pb[0], false, false);
      auto s1 = __builtin_amdgcn_permlane16_swap(pa[1], pb[1], false, false);
      st16(hbase + (size_t)(2 * mp + (g & 1)) * (112 * 1024) + (c0 + 2 * i + (g >> 1)) * 256,
           (u32x4){s0[0], s1[0], s0[1], s1[1]});
    }
  }
  {
    u32x2 qm[2];
#pragma unroll
    for (int mp = 0; mp < 2; ++mp) {
      f32x4 gt, up;
#pragma unroll
      for (int r = 0; r < 4; ++r) {
        auto sw = __builtin_amdgcn_permlane16_swap(__float_as_uint(am[2 * mp][r]), __float_as_uint(am[2 * mp + 1][r]), false, false);
        gt[r] = __uint_as_float(sw[0]); up[r] = __uint_as_float(sw[1]);
      }
      qm[mp] = swiglu4(gt, up);
    }
    auto s0 = __builtin_amdgcn_permlane32_swap(qm[0][0], qm[1][0], false, false);
    auto s1 = __builtin_amdgcn_permlane32_swap(qm[0][1], qm[1][1], false, false);
    st16(hbase + (size_t)g * (112 * 1024) + (nblk * 14 + 6 + wn) * 256, (u32x4){s0[0], s1[0], s0[1], s1[1]});
  }
  STAMP_END(2);
}

constexpr int G2_R = 6;
constexpr int G2_WOFF = 16384;
constexpr int G2_SLOT = 16384 + 8192;
constexpr int G2_NS = 56;

struct G2Frags { bf16x8 x[2]; bf16x8 w[4]; };

__global__ __launch_bounds__(512, 2) void k3_gemm2(
    const u16* __restrict__ hh, const u16* __restrict__ w2, const u16* __restrict__ x1,
    const float* __restrict__ modp, u16* __restrict__ out) {
  extern __shared__ __attribute__((aligned(16))) char smem[];
  const int t = threadIdx.x, lane = t & 63;
  const int w = __builtin_amdgcn_readfirstlane(t >> 6);
  const int b = blockIdx.x, xcd = b & 7, idx = b >> 3;
  STAMP_DECL
  const int mblk = (xcd & 3) * 4 + (idx & 3), nblk = (xcd >> 2) * 8 + (idx >> 2);
  const int wm = w & 3, wk = w >> 2;
  const u32 lbase = (u32)(uintptr_t)(LDSP char*)smem;

  const char* xsrc = (const char*)hh + (size_t)(mblk * 8 + w) * (112 * 1024);
  const char* wsrc = (const char*)w2 + nblk * 128;
  const u32 xv0 = lane * 16, xv1 = lane * 16 + 1024;
  u32 wv;
  {
    int pos = 8 * w + (lane >> 3);
    int pp = pos & 31;
    int k = (pos & 32) + 8 * ((pp >> 2) & 3) + 4 * (pp >> 4) + (pp & 3);
    int cc = (lane & 7) ^ (((pos >> 1) & 3) << 1);
    wv = k * 2048 + cc * 16;
  }
  const u32 xdst = lbase + w * 2048;
  const u32 wdst = lbase + G2_WOFF + w * 1024;
  auto issue = [&](int s, u32 boff) {
    const char* xs = xsrc + s * 2048;
    const char* ws = wsrc + (size_t)s * (64 * 2048);
    glds16(xs, xv0, xdst + boff);
    glds16(xs, xv1, xdst + boff + 1024);
    glds16(ws, wv, wdst + boff);
  };
  const int g = lane >> 4, q = (lane >> 2) & 3, p = lane & 3;
  const u32 xr = lbase + wm * 4096 + wk * 1024 + lane * 16;
  const int key = (2 * g + (q >> 1)) & 3;
  u32 wra[4];
#pragma unroll
  for (int nt = 0; nt < 4; ++nt)
    wra[nt] = lbase + G2_WOFF + wk * 4096 + (4 * g + q) * 128 + ((nt ^ key) * 32) + 8 * p;
  auto rd = [&](G2Frags& f, u32 boff) {
    f.x[0] = lds_b128(xr + boff);
    f.x[1] = lds_b128(xr + boff + 2048);
#pragma unroll
    for (int nt = 0; nt < 4; ++nt) f.w[nt] = lds_tr2(wra[nt] + boff, wra[nt] + boff + 2048);
  };
  f32x4 acc[2][4];
#pragma unroll
  for (int m = 0; m < 2; ++m)
#pragma unroll
    for (int nt = 0; nt < 4; ++nt) acc[m][nt] = (f32x4){0.f, 0.f, 0.f, 0.f};
  auto mm = [&](const G2Frags& f) {
#pragma unroll
    for (int m = 0; m < 2; ++m)
#pragma unroll
      for (int nt = 0; nt < 4; ++nt) acc[m][nt] = mfma16(f.w[nt], f.x[m], acc[m][nt]);
  };
  G2Frags fa, fb;
#pragma unroll
  for (int s = 0; s < G2_R; ++s) issue(s, s * G2_SLOT);
  STAMP(2);
  WAIT_VM(3 * (G2_R - 1));
  BARRIER();
  STAMP(3);
  rd(fa, 0);
  u32 cur = 0;
  auto nxt = [&](u32 o) { u32 n = o + G2_SLOT; return n == G2_R * G2_SLOT ? 0u : n; };
#pragma unroll 1
  for (int tt = 0; tt < G2_NS - G2_R; tt += 2) {
    u32 n1 = nxt(cur), n2 = nxt(n1);
    WAIT_VM(3 * (G2_R - 2)); BARRIER();
    issue(tt + G2_R, cur);
    rd(fb, n1);
    mm(fa);
    WAIT_VM(3 * (G2_R - 2)); BARRIER();
    issue(tt + 1 + G2_R, n1);
    rd(fa, n2);
    mm(fb);
    cur = n2;
  }
  STAMP(4);
  const int m16 = lane & 15;
  const int row = mblk * 128 + (2 * wm + wk) * 16 + m16;
  const int col0 = nblk * 64 + 4 * g;
  {
    u32 n1 = nxt(cur), n2 = nxt(n1), n3 = nxt(n2), n4 = nxt(n3), n5 = nxt(n4);
    WAIT_VM(12); BARRIER();
    rd(fb, n1); mm(fa);
    WAIT_VM(9); BARRIER();
    rd(fa, n2); mm(fb);
    WAIT_VM(6); BARRIER();
    rd(fb, n3); mm(fa);
    WAIT_VM(3); BARRIER();
    rd(fa, n4); mm(fb);
    WAIT_VM(0); BARRIER();
    rd(fb, n5); mm(fa);
    mm(fb);
  }
  STAMP(5);
  u32x2 xq[4], gq[4];
  f32x4 gp[4][2];
#pragma unroll
  for (int nt = 0; nt < 4; ++nt) {
    xq[nt] = *(const u32x2*)(x1 + (size_t)row * D_ + col0 + 16 * nt);
    gp[nt][0] = *(const f32x4*)(modp + 5 * 1024 + col0 + 16 * nt);
    gp[nt][1] = *(const f32x4*)(modp + 6144 + 5 * 1024 + col0 + 16 * nt);
  }
  BARRIER();
#pragma unroll
  for (int nt = 0; nt < 4; ++nt)
    *(LDSP f32x4*)(lbase + (w * 4 + nt) * 1024 + lane * 16) = wk ? acc[0][nt] : acc[1][nt];
  BARRIER();
#pragma unroll
  for (int nt = 0; nt < 4; ++nt) {
    f32x4 o = *(const LDSP f32x4*)(lbase + ((w ^ 4) * 4 + nt) * 1024 + lane * 16);
    f32x4 mine = wk ? acc[1][nt] : acc[0][nt];
    o += mine;
    { f32x4 gs = gp[nt][0] + gp[nt][1]; gq[nt] = (u32x2){pk_bf(gs[0], gs[1]), pk_bf(gs[2], gs[3])}; }
    u32 b0 = pk_bf(o[0], o[1]), b1 = pk_bf(o[2], o[3]);
    float r0 = fmaf(bf_lo(gq[nt][0]), bf_lo(b0), bf_lo(xq[nt][0]));
    float r1 = fmaf(bf_hi(gq[nt][0]), bf_hi(b0), bf_hi(xq[nt][0]));
    float r2 = fmaf(bf_lo(gq[nt][1]), bf_lo(b1), bf_lo(xq[nt][1]));
    float r3 = fmaf(bf_hi(gq[nt][1]), bf_hi(b1), bf_hi(xq[nt][1]));
    *(u32x2*)(out + (size_t)row * D_ + col0 + 16 * nt) = (u32x2){pk_bf(r0, r1), pk_bf(r2, r3)};
  }
  STAMP_END(3);
}

extern "C" void kernel_launch(void* const* d_in, const int* in_sizes, int n_in,
                              void* d_out, int out_size, void* d_ws, size_t ws_size,
                              hipStream_t stream) {
  const u16* x = (const u16*)d_in[0];
  const u16* kin = (const u16*)d_in[1];
  const u16* vin = (const u16*)d_in[2];
  const u16* shk = (const u16*)d_in[3];
  const u16* ssmk = (const u16*)d_in[4];
  const u16* ssmD = (const u16*)d_in[5];
  const u16* w1 = (const u16*)d_in[7];
  const u16* w3 = (const u16*)d_in[8];
  const u16* w2 = (const u16*)d_in[9];
  const u16* cvec = (const u16*)d_in[10];
  const u16* wmod = (const u16*)d_in[11];
  char* ws = (char*)d_ws;
  float* mod = (float*)(ws + WS_MOD);
  u16* yp = (u16*)(ws + WS_YP);
  u16* x1 = (u16*)(ws + WS_X1);
  u16* h2 = (u16*)(ws + WS_H2);
  u16* hh = (u16*)(ws + WS_HH);
  (void)hipFuncSetAttribute((const void*)k0_conv, hipFuncAttributeMaxDynamicSharedMemorySize, C_LDS);
  (void)hipFuncSetAttribute((const void*)k2_gemm1, hipFuncAttributeMaxDynamicSharedMemorySize, G1_R * G1_SLOT + 256);
  (void)hipFuncSetAttribute((const void*)k3_gemm2, hipFuncAttributeMaxDynamicSharedMemorySize, G2_R * G2_SLOT);
  k0_conv<<<256, 512, C_LDS, stream>>>(kin, vin, shk, ssmk, ssmD, cvec, wmod, mod, yp, x);
  k1_norm<<<256, 512, 0, stream>>>(x, yp, mod, x1, h2, w1, w3);
  k2_gemm1<<<256, 512, G1_R * G1_SLOT + 256, stream>>>(h2, w1, w3, hh, w2);
  k3_gemm2<<<256, 512, G2_R * G2_SLOT, stream>>>(hh, w2, x1, mod, (u16*)d_out);
}
```

```cpp
#include <hip/hip_runtime.h>
#include <stdint.h>

typedef unsigned short u16;
typedef unsigned int u32;
typedef short s16x4 __attribute__((ext_vector_type(4)));
typedef short s16x8 __attribute__((ext_vector_type(8)));
typedef __bf16 bf16x2 __attribute__((ext_vector_type(2)));
typedef __bf16 bf16x8 __attribute__((ext_vector_type(8)));
typedef float f32x2 __attribute__((ext_vector_type(2)));
typedef float f32x4 __attribute__((ext_vector_type(4)));
typedef u32 u32x2 __attribute__((ext_vector_type(2)));
typedef u32 u32x4 __attribute__((ext_vector_type(4)));
#define LDSP __attribute__((address_space(3)))

constexpr int S_ = 2048, D_ = 1024, F_ = 3584;

constexpr size_t WS_MOD = 0;
constexpr size_t WS_YP = 1u << 20;
constexpr size_t WS_X1 = WS_YP + (8u << 20);
constexpr size_t WS_H2 = WS_X1 + (4u << 20);
constexpr size_t WS_HH = WS_H2 + (4u << 20);

__device__ __forceinline__ float bf_lo(u32 v) { return __uint_as_float(v << 16); }
__device__ __forceinline__ float bf_hi(u32 v) { return __uint_as_float(v & 0xffff0000u); }
__device__ __forceinline__ u32 pk_bf(float lo, float hi) {
  f32x2 f = {lo, hi};
  bf16x2 b = __builtin_convertvector(f, bf16x2);
  return __builtin_bit_cast(u32, b);
}
__device__ __forceinline__ float rbf(float x) { return bf_lo(pk_bf(x, x)); }
__device__ __forceinline__ float silu_f(float x) {
  return x * __builtin_amdgcn_rcpf(1.0f + __builtin_amdgcn_exp2f(x * -1.44269504088896341f));
}
__device__ __forceinline__ f32x4 mfma16(bf16x8 a, bf16x8 b, f32x4 c) {
  return __builtin_amdgcn_mfma_f32_16x16x32_bf16(a, b, c, 0, 0, 0);
}
__device__ __forceinline__ void glds16(const void* sbase, u32 voff, u32 ldsoff) {
  asm volatile("s_mov_b32 m0, %2\n\ts_nop 0\n\tglobal_load_lds_dwordx4 %0, %1"
               :: "v"(voff), "s"(sbase), "s"(ldsoff) : "memory");
}
__device__ __forceinline__ void glds16_48(const void* sbase, u32 voff, u32 ldsoff) {
  asm volatile("s_mov_b32 m0, %2\n\ts_mov_b32 exec_hi, 0xffff\n\tglobal_load_lds_dwordx4 %0, %1\n\ts_mov_b32 exec_hi, -1"
               :: "v"(voff), "s"(sbase), "s"(ldsoff) : "memory");
}
__device__ __forceinline__ void glds4(const void* sbase, u32 voff, u32 ldsoff) {
  asm volatile("s_mov_b32 m0, %2\n\ts_nop 0\n\tglobal_load_lds_dword %0, %1"
               :: "v"(voff), "s"(sbase), "s"(ldsoff) : "memory");
}
__device__ __forceinline__ void st16(void* p, u32x4 v) {
  asm volatile("global_store_dwordx4 %0, %1, off sc1\n\ts_nop 1" :: "v"(p), "v"(v) : "memory");
}
__device__ __forceinline__ void st8(void* p, u32x2 v) {
  asm volatile("global_store_dwordx2 %0, %1, off sc1\n\ts_nop 1" :: "v"(p), "v"(v) : "memory");
}
#define SCHED0() __builtin_amdgcn_sched_barrier(0)
#define WAIT_VM(n) do { SCHED0(); asm volatile("s_waitcnt vmcnt(%0)" :: "n"(n) : "memory"); } while (0)
#define BARRIER() do { __builtin_amdgcn_s_waitcnt(0xc07f); asm volatile("s_barrier" ::: "memory"); SCHED0(); } while (0)

#define STAMP_DECL
#define STAMP(i)
#define STAMP_END(kid)

__device__ __forceinline__ bf16x8 lds_b128(u32 off) {
  return *(const LDSP bf16x8*)(off);
}
__device__ __forceinline__ bf16x8 lds_tr2(u32 off0, u32 off1) {
  s16x4 a = __builtin_amdgcn_ds_read_tr16_b64_v4i16((LDSP s16x4*)(off0));
  s16x4 b = __builtin_amdgcn_ds_read_tr16_b64_v4i16((LDSP s16x4*)(off1));
  s16x8 ab = __builtin_shufflevector(a, b, 0, 1, 2, 3, 4, 5, 6, 7);
  return __builtin_bit_cast(bf16x8, ab);
}

constexpr int C_KPAD = 448;
constexpr int C_KE = C_KPAD;
constexpr int C_KO = 4576 + C_KPAD;
constexpr int C_R1 = 9152;
constexpr int C_R0 = C_R1 + 2688;
constexpr int C_CH = 9152 + 5376;
constexpr int C_Y = 8 * C_CH;
constexpr int C_GP = C_Y + 32768;
constexpr int C_FL = C_GP + 4096;
constexpr int C_LDS = C_FL + 64;
#define K0_PRIO 3
#define K0_SLEEP 1
#define K0_PFB 0

template <typename T> __device__ __forceinline__ T ldnt(const void* p) {
  return __builtin_nontemporal_load((const T*)p);
}
__device__ __forceinline__ void lds_inc(u32 addr) {
  asm volatile("ds_add_u32 %0, %1" :: "v"(addr), "v"(1u) : "memory");
}
__device__ __forceinline__ void lds_wait(u32 addr, u32 val) {
  while (true) {
    u32 v = *(volatile const LDSP u32*)(addr);
    if ((u32)__builtin_amdgcn_readfirstlane(v) >= val) break;
    __builtin_amdgcn_s_sleep(K0_SLEEP);
  }
  asm volatile("" ::: "memory");
}
__device__ __forceinline__ f32x2 unpk(u32 v) { return (f32x2){bf_lo(v), bf_hi(v)}; }
__device__ __forceinline__ u32 shr1(u32 old, u32 v) {
  return __builtin_amdgcn_update_dpp(old, v, 0x138, 0xf, 0xf, false);
}
__device__ __forceinline__ bf16x8 lds_b64x2(u32 off) {
  u32x2 lo = *(volatile const LDSP u32x2*)(off);
  u32x2 hi = *(volatile const LDSP u32x2*)(off + 8);
  u32x4 v = {lo[0], lo[1], hi[0], hi[1]};
  return __builtin_bit_cast(bf16x8, v);
}

template <int E, int NE, int A0, int NA>
__device__ __forceinline__ void conv_stage(f32x4 (&acc)[8], u32 la, u32 lb) {
  constexpr int NM = NA + (NE == 2 ? (E + 1 + A0 + NA - 1 <= 7 ? NA : NA - 1) : 0);
  constexpr int NL = NA + 2 * NE;
  bf16x8 A[2][NA], B[2][NE];
  la += 512 * A0; lb -= 512 * (E + NE - 1);
  auto ld = [&](int bu, u32 la_, u32 lb_) {
#pragma unroll
    for (int e = 0; e < NE; ++e) B[bu][e] = lds_b64x2(lb_ + 512 * (NE - 1 - e));
#pragma unroll
    for (int a = 0; a < NA; ++a) A[bu][a] = lds_b128(la_ + 512 * a);
  };
  auto mm = [&](int bu) {
#pragma unroll
    for (int e = 0; e < NE; ++e)
#pragma unroll
      for (int a = 0; a < NA; ++a)
        if (E + e + A0 + a <= 7) acc[E + e + A0 + a] = mfma16(A[bu][a], B[bu][e], acc[E + e + A0 + a]);
  };
  auto il = [&]() {
#pragma unroll
    for (int i = 0; i < NM; ++i) {
      __builtin_amdgcn_sched_group_barrier(0x008, 1, 0);
      __builtin_amdgcn_sched_group_barrier(0x100, (NL + NM - 1) / NM, 0);
    }
    SCHED0();
  };
  ld(0, la, lb);
  SCHED0();
#pragma unroll 1
  for (int qp = 0; qp < 3; ++qp) {
    ld(1, la - 64, lb - 64);
    mm(0);
    il();
    ld(0, la - 128, lb - 128);
    mm(1);
    il();
    la -= 128; lb -= 128;
  }
  ld(1, la - 64, lb - 64);
  mm(0);
  il();
  mm(1);
}

template <int G, int NCH>
__device__ __forceinline__ void k0_loader(
    const u16* __restrict__ Kin, const u16* __restrict__ Vin, const u16* __restrict__ shk,
    const u16* __restrict__ cvec, const u16* __restrict__ wmod, float* __restrict__ modp,
    const u16* __restrict__ Xpf, int lw, int lane, int pr, int xcd, int idx, u32 lbase
    ) {
  constexpr int TOP = G ? 2047 : 895, S0 = G ? 768 : 0;
  constexpr int NW = G ? 8 : 16;
  const int d0 = pr * 8;
  const size_t ro = (size_t)(S0 + 128 * lw + 2 * lane) * D_ + d0;
  const u16* kp = Kin + ro;
  const u16* vp = Vin + ro;
  u32x4 L[4 * NCH], wq[NW], cq[NW / 8];
  auto ld = [&](int n) {
    const u16* src = ((n & 2) ? vp : kp) + (size_t)(n >> 2) * 512 * D_ + (n & 1) * D_;
    L[n] = ldnt<u32x4>(src);
  };
  const int wrow = G ? (pr & 3) * 256 + lw * 64 + (lane >> 3) * 8 : (pr & 1) * 512 + lw * 128 + (lane >> 3) * 16;
  const int wcol = G ? 4096 + (pr >> 2) * 64 : (pr >> 1) * 64;
  const u16* wp = wmod + (size_t)wrow * 6144 + wcol + (lane & 7) * 8;
  auto item = [&](int n) {
    if (n < 4 * NCH) ld(n);
    else if (n < 4 * NCH + NW / 8) {
#pragma unroll
      for (int i = 8 * (n - 4 * NCH); i < 8 * (n - 4 * NCH) + 8; ++i) wq[i] = ldnt<u32x4>(wp + (size_t)i * 6144);
    }
  };
  u32x4 hk[NCH][3];
#pragma unroll
  for (int j = 0; j < NCH; ++j)
#pragma unroll
    for (int r = 0; r < 3; ++r) {
      const u32 row = (!G && j == 0 && lw == 0) ? 0u : (u32)(S0 + 512 * j + 128 * lw - 3 + r);
      hk[j][r] = *(const u32x4*)(Kin + (size_t)row * D_ + d0);
    }
  u32x4 sk[4];
#pragma unroll
  for (int i = 0; i < 4; ++i) sk[i] = *(const u32x4*)(shk + d0 * 4 + 8 * i);
#pragma unroll
  for (int i = 0; i < NW / 8; ++i) cq[i] = *(const u32x4*)(cvec + wrow + 8 * i);
  SCHED0();
  __builtin_amdgcn_s_setprio(K0_PRIO);
  __builtin_amdgcn_s_barrier();
  SCHED0();
#pragma unroll
  for (int n = 0; n < 4; ++n) item(n);
  u32 pf = 0;
  if (K0_PFB == 1 && lw < 2) {
    const int r = idx * 32 + lw * 16 + (lane >> 2);
    const int row = r < 256 ? 512 + r : 1024 + r;
    pf = ldnt<u32>(((lane & 2) ? Vin : Kin) + (size_t)row * D_ + xcd * 128 + (lane & 1) * 64);
  }
  item(4); item(5);
  SCHED0();
  if (!G) {
#pragma unroll
    for (int r = 0; r < 3; ++r) {
      asm volatile("" : "+s"(hk[0][r]));
      if (lw == 0) hk[0][r] = (u32x4){0u, 0u, 0u, 0u};
    }
  }
  const u32 uw = lbase + C_R1 + (TOP - 1 - S0 - 128 * lw - 2 * lane) * 2;
#pragma unroll
  for (int j = 0; j < NCH; ++j) {
#pragma unroll
    for (int hp = 0; hp < 4; ++hp) {
      const f32x2 W0 = {bf_lo(sk[hp][0]), bf_lo(sk[hp][2])}, W1 = {bf_hi(sk[hp][0]), bf_hi(sk[hp][2])};
      const f32x2 W2 = {bf_lo(sk[hp][1]), bf_lo(sk[hp][3])}, W3 = {bf_hi(sk[hp][1]), bf_hi(sk[hp][3])};
      const u32 kA = L[4 * j][hp], kB = L[4 * j + 1][hp];
      const u32 kB1 = shr1(hk[j][2][hp], kB);
      const u32 kA1 = shr1(hk[j][1][hp], kA);
      const u32 kB2 = shr1(hk[j][0][hp], kB1);
      const f32x2 fA = unpk(kA), fB = unpk(kB), fB1 = unpk(kB1), fA1 = unpk(kA1), fB2 = unpk(kB2);
      f32x2 a0 = fB2 * W3, a1 = fA1 * W3;
      a0 = __builtin_elementwise_fma(fA1, W2, a0); a1 = __builtin_elementwise_fma(fB1, W2, a1);
      a0 = __builtin_elementwise_fma(fB1, W1, a0); a1 = __builtin_elementwise_fma(fA, W1, a1);
      a0 = __builtin_elementwise_fma(fA, W0, a0);  a1 = __builtin_elementwise_fma(fB, W0, a1);
      a0 *= unpk(L[4 * j + 2][hp]);
      a1 *= unpk(L[4 * j + 3][hp]);
#pragma unroll
      for (int c = 0; c < 2; ++c) {
        const u32 pk = pk_bf(a1[c], a0[c]);
        const u32 o = uw + (2 * hp + c) * C_CH - j * 1024;
        *(LDSP u32*)(o) = pk;
        *(LDSP u32*)(o + (C_R0 - C_R1) - 4) = pk;
      }
    }
    if (lane == 0) lds_inc(lbase + C_FL + 4 * (G ? j : (j == 0 && NCH == 2 ? 0 : 1)));
    SCHED0();
    STAMP(2 + j);
#pragma unroll
    for (int n = 4 * j + 6; n < 4 * j + 10; ++n) item(n);
    SCHED0();
  }
  u32 pf2 = *(const u32*)(Xpf + (size_t)blockIdx.x * 8192 + (lw * 64 + lane) * 32);
  u32 sc[NW / 2];
#pragma unroll
  for (int i = 0; i < NW / 2; ++i) {
    u32 cv = cq[i >> 2][i & 3];
    sc[i] = pk_bf(silu_f(bf_lo(cv)), silu_f(bf_hi(cv)));
  }
  float ga[8];
#pragma unroll
  for (int j = 0; j < 8; ++j) ga[j] = 0.f;
#pragma unroll
  for (int i = 0; i < NW / 2; ++i) {
#pragma unroll
    for (int j = 0; j < 4; ++j) {
      u32 a = wq[2 * i][j], bb = wq[2 * i + 1][j];
      u32 lo = __builtin_amdgcn_perm(bb, a, 0x05040100u);
      u32 hi = __builtin_amdgcn_perm(bb, a, 0x07060302u);
      ga[2 * j] = __builtin_amdgcn_fdot2_f32_bf16(__builtin_bit_cast(bf16x2, lo), __builtin_bit_cast(bf16x2, sc[i]), ga[2 * j], false);
      ga[2 * j + 1] = __builtin_amdgcn_fdot2_f32_bf16(__builtin_bit_cast(bf16x2, hi), __builtin_bit_cast(bf16x2, sc[i]), ga[2 * j + 1], false);
    }
  }
  STAMP(5);
#pragma unroll
  for (int j = 0; j < 8; ++j)
    ga[j] += __uint_as_float(__builtin_amdgcn_update_dpp(0u, __float_as_uint(ga[j]), 0x118, 0xf, 0xf, true));
  if (lane & 8) {
    u32 o = lbase + C_GP + ((lw * 4 + (lane >> 4)) * 64 + (lane & 7) * 8) * 4;
    *(LDSP f32x4*)(o) = (f32x4){ga[0], ga[1], ga[2], ga[3]};
    *(LDSP f32x4*)(o + 16) = (f32x4){ga[4], ga[5], ga[6], ga[7]};
  }
  u32 old = 0;
  if (lane == 0)
    asm volatile("ds_add_rtn_u32 %0, %1, %2\n\ts_waitcnt lgkmcnt(0)" : "=v"(old) : "v"(lbase + C_FL + 32), "v"(1u) : "memory");
  if (__builtin_amdgcn_readfirstlane(old) == 3) {
    float s = 0.f;
#pragma unroll
    for (int i = 0; i < 16; ++i) s += *(const LDSP float*)(lbase + C_GP + (i * 64 + lane) * 4);
    modp[(G ? (pr & 3) : (pr & 1)) * 6144 + wcol + lane] = s;
  }
  asm volatile("" :: "v"(pf), "v"(pf2));
}

template <int G>
__device__ __forceinline__ void k0_mfma(
    const u16* __restrict__ Kin, const u16* __restrict__ Vin, const u16* __restrict__ ssmk,
    const u16* __restrict__ ssmD, u16* __restrict__ yp, int w, int lane, int pr, int xcd, int idx, u32 lbase
    ) {
  constexpr int TOP = G ? 2047 : 895, S0 = G ? 768 : 0;
  constexpr int NF = G ? 3 : 4;
  u32 pf = 0;
  const u16* pfp;
  {
    const int r = idx * 32 + (w & 1) * 16 + (lane >> 2);
    const int row = (w & 2) ? (r < 256 ? 512 + r : 1024 + r) : (r < 512 ? r : 256 + r);
    pfp = ((lane & 2) ? Vin : Kin) + (size_t)row * D_ + xcd * 128 + (lane & 1) * 64;
  }
  if (w < 2) pf = ldnt<u32>(pfp);
  const u16* fk = ssmk + (size_t)(pr * 8 + w) * S_;
  u32x4 fc[NF];
#pragma unroll
  for (int i = 0; i < NF; ++i) fc[i] = ldnt<u32x4>(fk + (lane + 64 * i) * 8);
  if (K0_PFB == 0 && (w >> 1) == 1) pf = ldnt<u32>(pfp);
  const u32 dD = *(const u32*)(ssmD + pr * 8 + (w & 6));
  SCHED0();
  const u32 cb = lbase + w * C_CH;
  constexpr int Z = (TOP - S0 + 1) * 2;
  if (lane < 56) *(LDSP u32x4*)(cb + (lane / 28) * 4576 + (lane % 28) * 16) = (u32x4){0u, 0u, 0u, 0u};
  if (lane < 16) { if ((lane & 7) < 5) *(LDSP u32x4*)(cb + ((lane >> 3) ? C_R0 : C_R1) + Z - 16 + (lane & 7) * 16) = (u32x4){0u, 0u, 0u, 0u}; }
  else if (!G && lane < 32) *(LDSP u32x4*)(cb + ((lane & 8) ? C_R0 : C_R1) + 128 + (lane & 7) * 16) = (u32x4){0u, 0u, 0u, 0u};
  if (w == 0 && lane >= 48) *(LDSP u32*)(lbase + C_FL + 4 * (lane - 48)) = 0u;
  BARRIER();
  const float Dv = (w & 1) ? bf_hi(dD) : bf_lo(dD);
#pragma unroll
  for (int i = 0; i < NF; ++i) {
    const int z = lane + 64 * i;
    const u32 prev = shr1(i ? __builtin_amdgcn_readlane(fc[i ? i - 1 : 0][3], 63) : 0u, fc[i][3]);
    u32x4 e;
    e[0] = __builtin_amdgcn_alignbit(fc[i][0], prev, 16);
    e[1] = __builtin_amdgcn_alignbit(fc[i][1], fc[i][0], 16);
    e[2] = __builtin_amdgcn_alignbit(fc[i][2], fc[i][1], 16);
    e[3] = __builtin_amdgcn_alignbit(fc[i][3], fc[i][2], 16);
    *(LDSP u32x4*)(cb + C_KE + z * 16) = e;
    *(LDSP u32x4*)(cb + C_KO + z * 16) = fc[i];
  }
  const int g = lane >> 4, i16 = lane & 15;
  const u32 la = cb + ((i16 & 1) ? C_KO : C_KE) + 64 * (i16 >> 1) + 16 * g;
  const u32 lb = cb + ((i16 & 1) ? C_R1 : C_R0) + 2 * (TOP - 3) - 8 * (i16 >> 1) + 16 * g;
  f32x4 acc[8];
#pragma unroll
  for (int c = 0; c < 8; ++c) acc[c] = (f32x4){0.f, 0.f, 0.f, 0.f};
  const u32 ur = cb + C_R1 + (TOP - 1 - 64 * g - 2 * i16) * 2;
  const u32 yw = lbase + C_Y + w * 4096 + (64 * g + 2 * i16) * 2;
  const u32 sel = (lane & 1) ? 0x07060302u : 0x05040100u;
  auto finA = [&](int c0, int c1, int k) {
#pragma unroll
    for (int c = c0; c < c1; ++c) {
      if (G || c < 3) {
        const u32 u01 = *(const LDSP u32*)(ur - 512 * c);
        const u32 u23 = *(const LDSP u32*)(ur - 512 * c - 64);
        *(LDSP u32*)(yw + 512 * c) = pk_bf(fmaf(bf_hi(u01), Dv, acc[c][0]), fmaf(bf_lo(u01), Dv, acc[c][1]));
        *(LDSP u32*)(yw + 512 * c + 64) = pk_bf(fmaf(bf_hi(u23), Dv, acc[c][2]), fmaf(bf_lo(u23), Dv, acc[c][3]));
      } else {
        *(LDSP u32*)(yw + 512 * c) = pk_bf(acc[c][0], acc[c][1]);
        *(LDSP u32*)(yw + 512 * c + 64) = pk_bf(acc[c][2], acc[c][3]);
      }
    }
    if (lane == 0) lds_inc(lbase + C_FL + 16 + 4 * k);
  };
  auto finB = [&](int r0) {
    const int r = r0 + lane;
    u32 y[8];
#pragma unroll
    for (int ch = 0; ch < 8; ++ch) y[ch] = *(const LDSP u32*)(lbase + C_Y + ch * 4096 + (r & ~1) * 2);
    u32x4 o = {__builtin_amdgcn_perm(y[1], y[0], sel), __builtin_amdgcn_perm(y[3], y[2], sel),
               __builtin_amdgcn_perm(y[5], y[4], sel), __builtin_amdgcn_perm(y[7], y[6], sel)};
    st16(yp + (G ? S_ * D_ - 768 * D_ : 0) + ((size_t)(r >> 3) * 128 + pr) * 64 + (r & 7) * 8, o);
  };
  asm volatile("" :: "v"(pf));
  STAMP(2);
  if (!G) {
    lds_wait(lbase + C_FL + 0, 2);
    STAMP(3);
    conv_stage<0, 1, 0, 8>(acc, la, lb);
    finA(0, 1, 0);
    lds_wait(lbase + C_FL + 4, 4);
    STAMP(4);
    {
      const bf16x8 B = lds_b64x2(lb - 64 * 24);
#pragma unroll
      for (int a = 0; a < 5; ++a) acc[3 + a] = mfma16(lds_b128(la + 512 * a), B, acc[3 + a]);
    }
    conv_stage<1, 2, 0, 3>(acc, la, lb);
    finA(1, 4, 1);
    conv_stage<1, 2, 3, 4>(acc, la, lb);
    STAMP(5);
    finA(4, 8, 2);
    lds_wait(lbase + C_FL + 16, 8);
    lds_wait(lbase + C_FL + 20, 8);
    finB(128 * w); finB(128 * w + 64);
    lds_wait(lbase + C_FL + 24, 8);
    finB(1024 + 128 * w); finB(1024 + 128 * w + 64);
  } else {
    lds_wait(lbase + C_FL + 0, 4);
    STAMP(3);
    conv_stage<3, 2, 0, 5>(acc, la, lb);
    finA(3, 5, 0);
    lds_wait(lbase + C_FL + 4, 4);
    STAMP(4);
    conv_stage<5, 2, 0, 3>(acc, la, lb);
    finA(5, 7, 1);
    bf16x8 A[8];
#pragma unroll
    for (int q = 0; q < 8; ++q) A[q] = lds_b128(la - 64 * q);
    lds_wait(lbase + C_FL + 16, 8);
    finB(768 + 64 * w);
    lds_wait(lbase + C_FL + 8, 2);
    STAMP(5);
    {
      bf16x8 B[8];
#pragma unroll
      for (int q = 0; q < 8; ++q) B[q] = lds_b64x2(lb - 64 * (56 + q));
      f32x4 t2 = {0.f, 0.f, 0.f, 0.f};
#pragma unroll
      for (int q = 0; q < 8; q += 2) { acc[7] = mfma16(A[q], B[q], acc[7]); t2 = mfma16(A[q + 1], B[q + 1], t2); }
      acc[7] += t2;
    }
    finA(7, 8, 2);
    lds_wait(lbase + C_FL + 20, 8);
    finB(1280 + 64 * w);
    if (w < 4) {
      lds_wait(lbase + C_FL + 24, 8);
      finB(1792 + 64 * w);
    }
  }
}

#define ST_ARG
__global__ __launch_bounds__(768, 3) void k0_conv(
    const u16* __restrict__ Kin, const u16* __restrict__ Vin, const u16* __restrict__ shk,
    const u16* __restrict__ ssmk, const u16* __restrict__ ssmD, const u16* __restrict__ cvec,
    const u16* __restrict__ wmod, float* __restrict__ modp, u16* __restrict__ yp,
    const u16* __restrict__ Xpf) {
  extern __shared__ __attribute__((aligned(16))) char smem[];
  STAMP_DECL
  const int t = threadIdx.x, lane = t & 63;
  const int w = __builtin_amdgcn_readfirstlane(t >> 6);
  const int b = blockIdx.x, xcd = b & 7, idx = b >> 3;
  const int pr = xcd * 16 + (idx >> 1);
  const u32 lbase = (u32)(uintptr_t)(LDSP char*)smem;
  if (idx & 1) {
    if (w < 8) k0_mfma<1>(Kin, Vin, ssmk, ssmD, yp, w, lane, pr, xcd, idx, lbase ST_ARG);
    else if (w < 10) k0_loader<1, 3>(Kin, Vin, shk, cvec, wmod, modp, Xpf, w - 8, lane, pr, xcd, idx, lbase ST_ARG);
    else k0_loader<1, 2>(Kin, Vin, shk, cvec, wmod, modp, Xpf, w - 8, lane, pr, xcd, idx, lbase ST_ARG);
  } else {
    if (w < 8) k0_mfma<0>(Kin, Vin, ssmk, ssmD, yp, w, lane, pr, xcd, idx, lbase ST_ARG);
    else if (w < 10) k0_loader<0, 2>(Kin, Vin, shk, cvec, wmod, modp, Xpf, w - 8, lane, pr, xcd, idx, lbase ST_ARG);
    else k0_loader<0, 1>(Kin, Vin, shk, cvec, wmod, modp, Xpf, w - 8, lane, pr, xcd, idx, lbase ST_ARG);
  }
  STAMP_END(0);
}

__device__ __forceinline__ float wave_sum(float x) {
  x += __uint_as_float(__builtin_amdgcn_update_dpp(0u, __float_as_uint(x), 0x111, 0xf, 0xf, true));
  x += __uint_as_float(__builtin_amdgcn_update_dpp(0u, __float_as_uint(x), 0x112, 0xf, 0xf, true));
  x += __uint_as_float(__builtin_amdgcn_update_dpp(0u, __float_as_uint(x), 0x114, 0xf, 0xf, true));
  x += __uint_as_float(__builtin_amdgcn_update_dpp(0u, __float_as_uint(x), 0x118, 0xf, 0xf, true));
  x += __uint_as_float(__builtin_amdgcn_update_dpp(0u, __float_as_uint(x), 0x142, 0xa, 0xf, true));
  x += __uint_as_float(__builtin_amdgcn_update_dpp(0u, __float_as_uint(x), 0x143, 0xc, 0xf, true));
  return __uint_as_float(__builtin_amdgcn_readlane(__float_as_uint(x), 63));
}

constexpr int N_YRS = 2048 + 32;
constexpr int N_MOD = 8 * N_YRS;
constexpr int N_H = N_MOD + 10240;
constexpr int N_LDS = N_H + 16384;
#define K1_PFROWS 1

__global__ __launch_bounds__(512) void k1_norm(
    const u16* __restrict__ X, const u16* __restrict__ yp, const float* __restrict__ modp,
    u16* __restrict__ x1out, u16* __restrict__ h2out,
    const u16* __restrict__ w1pf, const u16* __restrict__ w3pf) {
  __shared__ __attribute__((aligned(16))) char smem[N_LDS];
  const int t = threadIdx.x, lane = t & 63;
  const int w = __builtin_amdgcn_readfirstlane(t >> 6);
  STAMP_DECL
  const int rb = blockIdx.x;
  const int row = rb * 8 + w;
  const u32 lbase = (u32)(uintptr_t)(LDSP char*)smem;
  u32x4 xq[2], yq[2], yq1[2];
  f32x4 mp[3][2], mp2[2];
  const bool two = rb >= 96;
#pragma unroll
  for (int i = 0; i < 2; ++i) {
    yq[i] = *(const u32x4*)(yp + (size_t)rb * 8192 + (t + 512 * i) * 8);
    if (two) yq1[i] = *(const u32x4*)(yp + (size_t)(rb + 160) * 8192 + (t + 512 * i) * 8);
  }
#pragma unroll
  for (int i = 0; i < 3; ++i)
    if (i < 2 || w < 4) {
      mp[i][0] = *(const f32x4*)(modp + (t + 512 * i) * 4);
      mp[i][1] = *(const f32x4*)(modp + 6144 + (t + 512 * i) * 4);
    }
  if (w < 4) {
    mp2[0] = *(const f32x4*)(modp + 2 * 6144 + (t + 1024) * 4);
    mp2[1] = *(const f32x4*)(modp + 3 * 6144 + (t + 1024) * 4);
  }
#pragma unroll
  for (int hf = 0; hf < 2; ++hf) xq[hf] = *(const u32x4*)(X + (size_t)row * D_ + hf * 512 + lane * 8);
  SCHED0();
  u32 pf = 0;
  if (w < 4 * K1_PFROWS) {
    const u16* src = ((w & 2) ? w3pf : w1pf) + (size_t)(rb + 256 * (w >> 2)) * F_ + ((w & 1) * 56 + (lane < 56 ? lane : 55)) * 32;
    pf = *(const u32*)src;
  }
  SCHED0();
#pragma unroll
  for (int i = 0; i < 2; ++i) {
    if (two) {
#pragma unroll
      for (int e = 0; e < 4; ++e) {
        const f32x2 sm = unpk(yq[i][e]) + unpk(yq1[i][e]);
        yq[i][e] = pk_bf(sm[0], sm[1]);
      }
    }
    *(LDSP u32x4*)(lbase + (t & 7) * N_YRS + ((t >> 3) + 64 * i) * 16) = yq[i];
  }
#pragma unroll
  for (int i = 0; i < 3; ++i)
    if (i < 2 || w < 4) {
      f32x4 s = mp[i][0] + mp[i][1];
      if (i == 2) s += mp2[0] + mp2[1];
      *(LDSP u32x2*)(lbase + N_MOD + (t + 512 * i) * 8) = (u32x2){pk_bf(s[0], s[1]), pk_bf(s[2], s[3])};
    }
  float x[16];
#pragma unroll
  for (int i = 0; i < 8; ++i) {
    u32 v = xq[i >> 2][i & 3];
    x[2 * i] = bf_lo(v); x[2 * i + 1] = bf_hi(v);
  }
  float s = 0.f;
#pragma unroll
  for (int i = 0; i < 16; ++i) s += x[i];
  const float mean = wave_sum(s) * (1.0f / 1024.0f);
  float q = 0.f;
#pragma unroll
  for (int i = 0; i < 16; ++i) { float d = x[i] - mean; q = fmaf(d, d, q); }
  const float var = wave_sum(q) * (1.0f / 1024.0f);
  const float rstd = __builtin_amdgcn_rsqf(var + 1e-5f);
  STAMP(2);
  BARRIER();
  STAMP(3);
  u32x4 yv[2], mq[5][2];
#pragma unroll
  for (int hf = 0; hf < 2; ++hf) yv[hf] = *(const LDSP u32x4*)(lbase + w * N_YRS + hf * 1024 + lane * 16);
#pragma unroll
  for (int m = 0; m < 5; ++m)
#pragma unroll
    for (int hf = 0; hf < 2; ++hf) mq[m][hf] = *(const LDSP u32x4*)(lbase + N_MOD + m * 2048 + hf * 1024 + lane * 16);
  float x1[16];
#pragma unroll
  for (int i = 0; i < 16; ++i) {
    const int hf = i >> 3, e = i & 7;
    u32 sa = mq[0][hf][e >> 1], ca = mq[1][hf][e >> 1], ga = mq[2][hf][e >> 1], yy = yv[hf][e >> 1];
    float saf = (e & 1) ? bf_hi(sa) : bf_lo(sa);
    float caf = (e & 1) ? bf_hi(ca) : bf_lo(ca);
    float gaf = (e & 1) ? bf_hi(ga) : bf_lo(ga);
    float yf = (e & 1) ? bf_hi(yy) : bf_lo(yy);
    float h = fmaf((x[i] - mean) * rstd, 1.0f + caf, saf);
    x1[i] = fmaf(gaf, h * yf, x[i]);
  }
  u32x4 xo[2];
#pragma unroll
  for (int i = 0; i < 8; ++i) {
    u32 p = pk_bf(x1[2 * i], x1[2 * i + 1]);
    xo[i >> 2][i & 3] = p;
    x1[2 * i] = bf_lo(p); x1[2 * i + 1] = bf_hi(p);
  }
#pragma unroll
  for (int hf = 0; hf < 2; ++hf) st16(x1out + (size_t)row * D_ + hf * 512 + lane * 8, xo[hf]);
  float s2 = 0.f;
#pragma unroll
  for (int i = 0; i < 16; ++i) s2 += x1[i];
  const float mean2 = wave_sum(s2) * (1.0f / 1024.0f);
  float q2 = 0.f;
#pragma unroll
  for (int i = 0; i < 16; ++i) { float d = x1[i] - mean2; q2 = fmaf(d, d, q2); }
  const float var2 = wave_sum(q2) * (1.0f / 1024.0f);
  const float rstd2 = __builtin_amdgcn_rsqf(var2 + 1e-5f);
#pragma unroll
  for (int hf = 0; hf < 2; ++hf) {
    u32x4 ho;
#pragma unroll
    for (int e = 0; e < 4; ++e) {
      const int i = hf * 4 + e;
      u32 sb = mq[3][hf][e], cb = mq[4][hf][e];
      float h0 = fmaf((x1[2 * i] - mean2) * rstd2, 1.0f + bf_lo(cb), bf_lo(sb));
      float h1 = fmaf((x1[2 * i + 1] - mean2) * rstd2, 1.0f + bf_hi(cb), bf_hi(sb));
      ho[e] = pk_bf(h0, h1);
    }
    *(LDSP u32x4*)(lbase + N_H + (hf * 64 + lane) * 128 + ((w ^ (lane >> 1)) & 7) * 16) = ho;
  }
  STAMP(4);
  BARRIER();
#pragma unroll
  for (int i = 0; i < 2; ++i) {
    const int chunk = (t >> 3) + 64 * i, r = t & 7;
    u32x4 v = *(const LDSP u32x4*)(lbase + N_H + chunk * 128 + ((r ^ (chunk >> 1)) & 7) * 16);
    st16((char*)h2out + (size_t)(rb >> 1) * 32768 + chunk * 256 + (rb & 1) * 128 + r * 16, v);
  }
  asm volatile("" :: "v"(pf));
  STAMP_END(1);
}

constexpr int G1_R = 5;
constexpr int G1_WOFF = 16384;
constexpr int G1_UP = 7168 + 16;
constexpr int G1_SLOT = 16384 + G1_UP + 7168;
constexpr int G1_NK = 32;

struct G1Frags { bf16x8 x[4]; bf16x8 wg[3], wu[3], wm; };

__global__ __launch_bounds__(512, 2) void k2_gemm1(
    const u16* __restrict__ h2, const u16* __restrict__ w1, const u16* __restrict__ w3,
    u16* __restrict__ hh, const u16* __restrict__ w2pf) {
  extern __shared__ __attribute__((aligned(16))) char smem[];
  const int t = threadIdx.x, lane = t & 63;
  const int w = __builtin_amdgcn_readfirstlane(t >> 6);
  const int b = blockIdx.x, xcd = b & 7, idx = b >> 3;
  STAMP_DECL
  const int mblk = (xcd & 1) * 4 + (idx & 3), nblk = (xcd >> 1) * 8 + (idx >> 2);
  const int wm = w & 3, wn = w >> 2;
  const u32 lbase = (u32)(uintptr_t)(LDSP char*)smem;

  const char* xsrc = (const char*)h2 + (size_t)(mblk * 16 + 2 * w) * 32768;
  const char* wsrc = (const char*)((w >> 2) ? w3 : w1) + nblk * 224;
  const u32 xv0 = lane * 16, xv1 = lane * 16 + 32768;
  u32 wv0, wv1;
  {
    int c0 = lane, c1 = lane + 64;
    if (c1 > 111) c1 = 111;
    int p0 = 8 * (w & 3) + c0 / 14, p1 = 8 * (w & 3) + c1 / 14;
    int k0 = 8 * ((p0 >> 2) & 3) + 4 * (p0 >> 4) + (p0 & 3);
    int k1 = 8 * ((p1 >> 2) & 3) + 4 * (p1 >> 4) + (p1 & 3);
    wv0 = k0 * 7168 + (c0 % 14) * 16;
    wv1 = k1 * 7168 + (c1 % 14) * 16;
  }
  const u32 xdst = lbase + 2 * w * 1024;
  const u32 wdst = lbase + G1_WOFF + (w >> 2) * G1_UP + (w & 3) * 1792;
  auto issue = [&](int kb, u32 boff) {
    const char* xs = xsrc + kb * 1024;
    const char* ws = wsrc + (size_t)kb * (32 * 7168);
    glds16(xs, xv0, xdst + boff);
    glds16(xs, xv1, xdst + boff + 1024);
    glds16(ws, wv0, wdst + boff);
    glds16_48(ws, wv1, wdst + boff + 1024);
  };
  const int g = lane >> 4, q = (lane >> 2) & 3, p = lane & 3;
  const u32 xr = lbase + wm * 4096 + lane * 16;
  const u32 wr = lbase + G1_WOFF + (4 * g + q) * 224 + 8 * p + wn * 128;
  const u32 wrm = lbase + G1_WOFF + (4 * g + q) * 224 + (p & 1) * G1_UP + (48 + 8 * wn + 4 * (p >> 1)) * 2;
  auto rd = [&](G1Frags& f, u32 boff) {
#pragma unroll
    for (int i = 0; i < 4; ++i) f.x[i] = lds_b128(xr + boff + i * 1024);
#pragma unroll
    for (int i = 0; i < 3; ++i) {
      f.wg[i] = lds_tr2(wr + boff + 32 * i, wr + boff + 32 * i + 3584);
      f.wu[i] = lds_tr2(wr + boff + G1_UP + 32 * i, wr + boff + G1_UP + 32 * i + 3584);
    }
    f.wm = lds_tr2(wrm + boff, wrm + boff + 3584);
  };
  f32x4 ag[4][3], au[4][3], am[4];
#pragma unroll
  for (int m = 0; m < 4; ++m) {
#pragma unroll
    for (int i = 0; i < 3; ++i) { ag[m][i] = (f32x4){0.f, 0.f, 0.f, 0.f}; au[m][i] = (f32x4){0.f, 0.f, 0.f, 0.f}; }
    am[m] = (f32x4){0.f, 0.f, 0.f, 0.f};
  }
  auto mm = [&](const G1Frags& f) {
#pragma unroll
    for (int m = 0; m < 4; ++m) {
#pragma unroll
      for (int i = 0; i < 3; ++i) {
        ag[m][i] = mfma16(f.wg[i], f.x[m], ag[m][i]);
        au[m][i] = mfma16(f.wu[i], f.x[m], au[m][i]);
      }
      am[m] = mfma16(f.wm, f.x[m], am[m]);
    }
  };

  G1Frags fa, fb;
  issue(0, 0);
  SCHED0();
  __builtin_amdgcn_s_barrier();
  SCHED0();
#pragma unroll
  for (int s = 1; s < G1_R; ++s) issue(s, s * G1_SLOT);
  glds4(w2pf, (u32)((b * 8 + w) * 56 + (lane < 56 ? lane : 55)) * 64u, lbase + G1_R * G1_SLOT);
  STAMP(2);
  WAIT_VM(4 * (G1_R - 1) + 1);
  BARRIER();
  STAMP(3);
  rd(fa, 0);
  if (wn) __builtin_amdgcn_s_setprio(1);
  u32 cur = 0;
  auto nxt = [&](u32 o) { u32 n = o + G1_SLOT; return n == G1_R * G1_SLOT ? 0u : n; };
  auto il = [&]() {
#pragma unroll
    for (int i = 0; i < 18; ++i) {
      __builtin_amdgcn_sched_group_barrier(0x008, 1, 0);
      __builtin_amdgcn_sched_group_barrier(0x100, 1, 0);
    }
    __builtin_amdgcn_sched_group_barrier(0x008, 10, 0);
    SCHED0();
  };
  auto step = [&](G1Frags& fc, G1Frags& fn, int kb, u32 bc, u32 bn) {
    if (!wn) issue(kb, bc);
    SCHED0();
    rd(fn, bn);
    mm(fc);
    il();
    if (wn) issue(kb, bc);
    SCHED0();
  };
  auto step0 = [&](G1Frags& fc, G1Frags& fn, u32 bn) {
    rd(fn, bn);
    mm(fc);
    il();
  };
#pragma unroll 1
  for (int tt = 0; tt < G1_NK - G1_R - 1; tt += 2) {
    u32 n1 = nxt(cur), n2 = nxt(n1);
    WAIT_VM(4 * (G1_R - 2)); BARRIER();
    step(fa, fb, tt + G1_R, cur, n1);
    WAIT_VM(4 * (G1_R - 2)); BARRIER();
    step(fb, fa, tt + 1 + G1_R, n1, n2);
    cur = n2;
  }
  STAMP(4);
  {
    u32 n1 = nxt(cur), n2 = nxt(n1), n3 = nxt(n2), n4 = nxt(n3);
    WAIT_VM(12); BARRIER();
    step(fa, fb, G1_NK - 1, cur, n1);
    WAIT_VM(12); BARRIER();
    step0(fb, fa, n2);
    WAIT_VM(8); BARRIER();
    step0(fa, fb, n3);
    WAIT_VM(4); BARRIER();
    step0(fb, fa, n4);
    WAIT_VM(0); BARRIER();
    step0(fa, fb, cur);
    mm(fb);
  }

  STAMP(5);
  const int m16 = lane & 15;
  char* hbase = (char*)hh + (size_t)(mblk * 16 + wm * 4) * (112 * 1024) + m16 * 16;
  const int c0 = nblk * 14 + wn * 8;
  auto swiglu4 = [&](const f32x4& gt, const f32x4& up) {
    float o[4];
#pragma unroll
    for (int r = 0; r < 4; ++r) o[r] = silu_f(gt[r]) * up[r];
    return (u32x2){pk_bf(o[0], o[1]), pk_bf(o[2], o[3])};
  };
#pragma unroll
  for (int mp = 0; mp < 2; ++mp) {
#pragma unroll
    for (int i = 0; i < 3; ++i) {
      u32x2 pa = swiglu4(ag[2 * mp][i], au[2 * mp][i]);
      u32x2 pb = swiglu4(ag[2 * mp + 1][i], au[2 * mp + 1][i]);
      auto s0 = __builtin_amdgcn_permlane16_swap(pa[0], pb[0], false, false);
      auto s1 = __builtin_amdgcn_permlane16_swap(pa[1], pb[1], false, false);
      st16(hbase + (size_t)(2 * mp + (g & 1)) * (112 * 1024) + (c0 + 2 * i + (g >> 1)) * 256,
           (u32x4){s0[0], s1[0], s0[1], s1[1]});
    }
  }
  {
    u32x2 qm[2];
#pragma unroll
    for (int mp = 0; mp < 2; ++mp) {
      f32x4 gt, up;
#pragma unroll
      for (int r = 0; r < 4; ++r) {
        auto sw = __builtin_amdgcn_permlane16_swap(__float_as_uint(am[2 * mp][r]), __float_as_uint(am[2 * mp + 1][r]), false, false);
        gt[r] = __uint_as_float(sw[0]); up[r] = __uint_as_float(sw[1]);
      }
      qm[mp] = swiglu4(gt, up);
    }
    auto s0 = __builtin_amdgcn_permlane32_swap(qm[0][0], qm[1][0], false, false);
    auto s1 = __builtin_amdgcn_permlane32_swap(qm[0][1], qm[1][1], false, false);
    st16(hbase + (size_t)g * (112 * 1024) + (nblk * 14 + 6 + wn) * 256, (u32x4){s0[0], s1[0], s0[1], s1[1]});
  }
  STAMP_END(2);
}

constexpr int G2_R = 6;
constexpr int G2_WOFF = 16384;
constexpr int G2_SLOT = 16384 + 8192;
constexpr int G2_NS = 56;

struct G2Frags { bf16x8 x[2]; bf16x8 w[4]; };

__global__ __launch_bounds__(512, 2) void k3_gemm2(
    const u16* __restrict__ hh, const u16* __restrict__ w2, const u16* __restrict__ x1,
    const float* __restrict__ modp, u16* __restrict__ out) {
  extern __shared__ __attribute__((aligned(16))) char smem[];
  const int t = threadIdx.x, lane = t & 63;
  const int w = __builtin_amdgcn_readfirstlane(t >> 6);
  const int b = blockIdx.x, xcd = b & 7, idx = b >> 3;
  STAMP_DECL
  const int mblk = (xcd & 3) * 4 + (idx & 3), nblk = (xcd >> 2) * 8 + (idx >> 2);
  const int wm = w & 3, wk = w >> 2;
  const u32 lbase = (u32)(uintptr_t)(LDSP char*)smem;

  const char* xsrc = (const char*)hh + (size_t)(mblk * 8 + w) * (112 * 1024);
  const char* wsrc = (const char*)w2 + nblk * 128;
  const u32 xv0 = lane * 16, xv1 = lane * 16 + 1024;
  u32 wv;
  {
    int pos = 8 * w + (lane >> 3);
    int pp = pos & 31;
    int k = (pos & 32) + 8 * ((pp >> 2) & 3) + 4 * (pp >> 4) + (pp & 3);
    int cc = (lane & 7) ^ (((pos >> 1) & 3) << 1);
    wv = k * 2048 + cc * 16;
  }
  const u32 xdst = lbase + w * 2048;
  const u32 wdst = lbase + G2_WOFF + w * 1024;
  auto issue = [&](int s, u32 boff) {
    const char* xs = xsrc + s * 2048;
    const char* ws = wsrc + (size_t)s * (64 * 2048);
    glds16(xs, xv0, xdst + boff);
    glds16(xs, xv1, xdst + boff + 1024);
    glds16(ws, wv, wdst + boff);
  };
  const int g = lane >> 4, q = (lane >> 2) & 3, p = lane & 3;
  const u32 xr = lbase + wm * 4096 + wk * 1024 + lane * 16;
  const int key = (2 * g + (q >> 1)) & 3;
  u32 wra[4];
#pragma unroll
  for (int nt = 0; nt < 4; ++nt)
    wra[nt] = lbase + G2_WOFF + wk * 4096 + (4 * g + q) * 128 + ((nt ^ key) * 32) + 8 * p;
  auto rd = [&](G2Frags& f, u32 boff) {
    f.x[0] = lds_b128(xr + boff);
    f.x[1] = lds_b128(xr + boff + 2048);
#pragma unroll
    for (int nt = 0; nt < 4; ++nt) f.w[nt] = lds_tr2(wra[nt] + boff, wra[nt] + boff + 2048);
  };
  f32x4 acc[2][4];
#pragma unroll
  for (int m = 0; m < 2; ++m)
#pragma unroll
    for (int nt = 0; nt < 4; ++nt) acc[m][nt] = (f32x4){0.f, 0.f, 0.f, 0.f};
  auto mm = [&](const G2Frags& f) {
#pragma unroll
    for (int m = 0; m < 2; ++m)
#pragma unroll
      for (int nt = 0; nt < 4; ++nt) acc[m][nt] = mfma16(f.w[nt], f.x[m], acc[m][nt]);
  };
  G2Frags fa, fb;
#pragma unroll
  for (int s = 0; s < G2_R; ++s) issue(s, s * G2_SLOT);
  STAMP(2);
  WAIT_VM(3 * (G2_R - 1));
  BARRIER();
  STAMP(3);
  rd(fa, 0);
  u32 cur = 0;
  auto nxt = [&](u32 o) { u32 n = o + G2_SLOT; return n == G2_R * G2_SLOT ? 0u : n; };
#pragma unroll 1
  for (int tt = 0; tt < G2_NS - G2_R; tt += 2) {
    u32 n1 = nxt(cur), n2 = nxt(n1);
    WAIT_VM(3 * (G2_R - 2)); BARRIER();
    issue(tt + G2_R, cur);
    rd(fb, n1);
    mm(fa);
    WAIT_VM(3 * (G2_R - 2)); BARRIER();
    issue(tt + 1 + G2_R, n1);
    rd(fa, n2);
    mm(fb);
    cur = n2;
  }
  STAMP(4);
  const int m16 = lane & 15;
  const int row = mblk * 128 + (2 * wm + wk) * 16 + m16;
  const int col0 = nblk * 64 + 4 * g;
  {
    u32 n1 = nxt(cur), n2 = nxt(n1), n3 = nxt(n2), n4 = nxt(n3), n5 = nxt(n4);
    WAIT_VM(12); BARRIER();
    rd(fb, n1); mm(fa);
    WAIT_VM(9); BARRIER();
    rd(fa, n2); mm(fb);
    WAIT_VM(6); BARRIER();
    rd(fb, n3); mm(fa);
    WAIT_VM(3); BARRIER();
    rd(fa, n4); mm(fb);
    WAIT_VM(0); BARRIER();
    rd(fb, n5); mm(fa);
    mm(fb);
  }
  STAMP(5);
  u32x2 xq[4], gq[4];
  f32x4 gp[4][4];
#pragma unroll
  for (int nt = 0; nt < 4; ++nt) {
    xq[nt] = *(const u32x2*)(x1 + (size_t)row * D_ + col0 + 16 * nt);
#pragma unroll
    for (int h = 0; h < 4; ++h) gp[nt][h] = *(const f32x4*)(modp + h * 6144 + 5 * 1024 + col0 + 16 * nt);
  }
  BARRIER();
#pragma unroll
  for (int nt = 0; nt < 4; ++nt)
    *(LDSP f32x4*)(lbase + (w * 4 + nt) * 1024 + lane * 16) = wk ? acc[0][nt] : acc[1][nt];
  BARRIER();
#pragma unroll
  for (int nt = 0; nt < 4; ++nt) {
    f32x4 o = *(const LDSP f32x4*)(lbase + ((w ^ 4) * 4 + nt) * 1024 + lane * 16);
    f32x4 mine = wk ? acc[1][nt] : acc[0][nt];
    o += mine;
    { f32x4 gs = (gp[nt][0] + gp[nt][1]) + (gp[nt][2] + gp[nt][3]); gq[nt] = (u32x2){pk_bf(gs[0], gs[1]), pk_bf(gs[2], gs[3])}; }
    u32 b0 = pk_bf(o[0], o[1]), b1 = pk_bf(o[2], o[3]);
    float r0 = fmaf(bf_lo(gq[nt][0]), bf_lo(b0), bf_lo(xq[nt][0]));
    float r1 = fmaf(bf_hi(gq[nt][0]), bf_hi(b0), bf_hi(xq[nt][0]));
    float r2 = fmaf(bf_lo(gq[nt][1]), bf_lo(b1), bf_lo(xq[nt][1]));
    float r3 = fmaf(bf_hi(gq[nt][1]), bf_hi(b1), bf_hi(xq[nt][1]));
    *(u32x2*)(out + (size_t)row * D_ + col0 + 16 * nt) = (u32x2){pk_bf(r0, r1), pk_bf(r2, r3)};
  }
  STAMP_END(3);
}

extern "C" void kernel_launch(void* const* d_in, const int* in_sizes, int n_in,
                              void* d_out, int out_size, void* d_ws, size_t ws_size,
                              hipStream_t stream) {
  const u16* x = (const u16*)d_in[0];
  const u16* kin = (const u16*)d_in[1];
  const u16* vin = (const u16*)d_in[2];
  const u16* shk = (const u16*)d_in[3];
  const u16* ssmk = (const u16*)d_in[4];
  const u16* ssmD = (const u16*)d_in[5];
  const u16* w1 = (const u16*)d_in[7];
  const u16* w3 = (const u16*)d_in[8];
  const u16* w2 = (const u16*)d_in[9];
  const u16* cvec = (const u16*)d_in[10];
  const u16* wmod = (const u16*)d_in[11];
  char* ws = (char*)d_ws;
  float* mod = (float*)(ws + WS_MOD);
  u16* yp = (u16*)(ws + WS_YP);
  u16* x1 = (u16*)(ws + WS_X1);
  u16* h2 = (u16*)(ws + WS_H2);
  u16* hh = (u16*)(ws + WS_HH);
  (void)hipFuncSetAttribute((const void*)k0_conv, hipFuncAttributeMaxDynamicSharedMemorySize, C_LDS);
  (void)hipFuncSetAttribute((const void*)k2_gemm1, hipFuncAttributeMaxDynamicSharedMemorySize, G1_R * G1_SLOT + 256);
  (void)hipFuncSetAttribute((const void*)k3_gemm2, hipFuncAttributeMaxDynamicSharedMemorySize, G2_R * G2_SLOT);
  k0_conv<<<256, 768, C_LDS, stream>>>(kin, vin, shk, ssmk, ssmD, cvec, wmod, mod, yp, x);
  k1_norm<<<256, 512, 0, stream>>>(x, yp, mod, x1, h2, w1, w3);
  k2_gemm1<<<256, 512, G1_R * G1_SLOT + 256, stream>>>(h2, w1, w3, hh, w2);
  k3_gemm2<<<256, 512, G2_R * G2_SLOT, stream>>>(hh, w2, x1, mod, (u16*)d_out);
}
```

```cpp
#include <hip/hip_runtime.h>
#include <stdint.h>

typedef unsigned short u16;
typedef unsigned int u32;
typedef short s16x4 __attribute__((ext_vector_type(4)));
typedef short s16x8 __attribute__((ext_vector_type(8)));
typedef __bf16 bf16x2 __attribute__((ext_vector_type(2)));
typedef __bf16 bf16x8 __attribute__((ext_vector_type(8)));
typedef float f32x2 __attribute__((ext_vector_type(2)));
typedef float f32x4 __attribute__((ext_vector_type(4)));
typedef u32 u32x2 __attribute__((ext_vector_type(2)));
typedef u32 u32x4 __attribute__((ext_vector_type(4)));
#define LDSP __attribute__((address_space(3)))

constexpr int S_ = 2048, D_ = 1024, F_ = 3584;

constexpr size_t WS_MOD = 0;
constexpr size_t WS_YP = 1u << 20;
constexpr size_t WS_X1 = WS_YP + (8u << 20);
constexpr size_t WS_H2 = WS_X1 + (4u << 20);
constexpr size_t WS_HH = WS_H2 + (4u << 20);
constexpr size_t WS_GB = 128u << 10;
constexpr size_t WS_XB = WS_HH + (16u << 20);

__device__ __forceinline__ float bf_lo(u32 v) { return __uint_as_float(v << 16); }
__device__ __forceinline__ float bf_hi(u32 v) { return __uint_as_float(v & 0xffff0000u); }
__device__ __forceinline__ u32 pk_bf(float lo, float hi) {
  f32x2 f = {lo, hi};
  bf16x2 b = __builtin_convertvector(f, bf16x2);
  return __builtin_bit_cast(u32, b);
}
__device__ __forceinline__ float rbf(float x) { return bf_lo(pk_bf(x, x)); }
__device__ __forceinline__ float silu_f(float x) {
  return x * __builtin_amdgcn_rcpf(1.0f + __builtin_amdgcn_exp2f(x * -1.44269504088896341f));
}
__device__ __forceinline__ f32x4 mfma16(bf16x8 a, bf16x8 b, f32x4 c) {
  return __builtin_amdgcn_mfma_f32_16x16x32_bf16(a, b, c, 0, 0, 0);
}
__device__ __forceinline__ void glds16(const void* sbase, u32 voff, u32 ldsoff) {
  asm volatile("s_mov_b32 m0, %2\n\ts_nop 0\n\tglobal_load_lds_dwordx4 %0, %1"
               :: "v"(voff), "s"(sbase), "s"(ldsoff) : "memory");
}
__device__ __forceinline__ void glds16_48(const void* sbase, u32 voff, u32 ldsoff) {
  asm volatile("s_mov_b32 m0, %2\n\ts_mov_b32 exec_hi, 0xffff\n\tglobal_load_lds_dwordx4 %0, %1\n\ts_mov_b32 exec_hi, -1"
               :: "v"(voff), "s"(sbase), "s"(ldsoff) : "memory");
}
__device__ __forceinline__ void glds4(const void* sbase, u32 voff, u32 ldsoff) {
  asm volatile("s_mov_b32 m0, %2\n\ts_nop 0\n\tglobal_load_lds_dword %0, %1"
               :: "v"(voff), "s"(sbase), "s"(ldsoff) : "memory");
}
__device__ __forceinline__ void st16(void* p, u32x4 v) {
  asm volatile("global_store_dwordx4 %0, %1, off sc1\n\ts_nop 1" :: "v"(p), "v"(v) : "memory");
}
__device__ __forceinline__ void st8(void* p, u32x2 v) {
  asm volatile("global_store_dwordx2 %0, %1, off sc1\n\ts_nop 1" :: "v"(p), "v"(v) : "memory");
}
__device__ __forceinline__ void gld16(u32x4& d, const void* sbase, u32 voff) {
  asm volatile("global_load_dwordx4 %0, %1, %2" : "=v"(d) : "v"(voff), "s"(sbase) : "memory");
}
__device__ __forceinline__ void gld16_sc1(u32x4& d, const void* sbase, u32 voff) {
  asm volatile("global_load_dwordx4 %0, %1, %2 sc1" : "=v"(d) : "v"(voff), "s"(sbase) : "memory");
}

#define SCHED0() __builtin_amdgcn_sched_barrier(0)
#define WAIT_VM(n) do { SCHED0(); asm volatile("s_waitcnt vmcnt(%0)" :: "n"(n) : "memory"); } while (0)
#define BARRIER() do { __builtin_amdgcn_s_waitcnt(0xc07f); asm volatile("s_barrier" ::: "memory"); SCHED0(); } while (0)

#define STAMP_DECL
#define STAMP(i)
#define STAMP_END(kid)

__device__ __forceinline__ bf16x8 lds_b128(u32 off) {
  return *(const LDSP bf16x8*)(off);
}
__device__ __forceinline__ bf16x8 lds_tr2(u32 off0, u32 off1) {
  s16x4 a = __builtin_amdgcn_ds_read_tr16_b64_v4i16((LDSP s16x4*)(off0));
  s16x4 b = __builtin_amdgcn_ds_read_tr16_b64_v4i16((LDSP s16x4*)(off1));
  s16x8 ab = __builtin_shufflevector(a, b, 0, 1, 2, 3, 4, 5, 6, 7);
  return __builtin_bit_cast(bf16x8, ab);
}

constexpr int C_KPAD = 448;
constexpr int C_KE = C_KPAD;
constexpr int C_KO = 4576 + C_KPAD;
constexpr int C_R1 = 9152;
constexpr int C_R0 = C_R1 + 2688;
constexpr int C_CH = 9152 + 5376;
constexpr int C_Y = 8 * C_CH;
constexpr int C_GP = C_Y + 32768;
constexpr int C_FL = C_GP + 4096;
constexpr int C_LDS = C_FL + 64;
#define K0_PRIO 3
#define K0_SLEEP 1
#define K0_WPF 4
#define K0_PFB 0

template <typename T> __device__ __forceinline__ T ldnt(const void* p) {
  return __builtin_nontemporal_load((const T*)p);
}
__device__ __forceinline__ void lds_inc(u32 addr) {
  asm volatile("ds_add_u32 %0, %1" :: "v"(addr), "v"(1u) : "memory");
}
__device__ __forceinline__ void lds_wait(u32 addr, u32 val) {
  while (true) {
    u32 v = *(volatile const LDSP u32*)(addr);
    if ((u32)__builtin_amdgcn_readfirstlane(v) >= val) break;
    __builtin_amdgcn_s_sleep(K0_SLEEP);
  }
  asm volatile("" ::: "memory");
}
__device__ __forceinline__ f32x2 unpk(u32 v) { return (f32x2){bf_lo(v), bf_hi(v)}; }
__device__ __forceinline__ u32 shr1(u32 old, u32 v) {
  return __builtin_amdgcn_update_dpp(old, v, 0x138, 0xf, 0xf, false);
}
__device__ __forceinline__ bf16x8 lds_b64x2(u32 off) {
  u32x2 lo = *(volatile const LDSP u32x2*)(off);
  u32x2 hi = *(volatile const LDSP u32x2*)(off + 8);
  u32x4 v = {lo[0], lo[1], hi[0], hi[1]};
  return __builtin_bit_cast(bf16x8, v);
}

template <int E, int NE, int A0, int NA>
__device__ __forceinline__ void conv_stage(f32x4 (&acc)[8], u32 la, u32 lb) {
  constexpr int NM = NA + (NE == 2 ? (E + 1 + A0 + NA - 1 <= 7 ? NA : NA - 1) : 0);
  constexpr int NL = NA + 2 * NE;
  bf16x8 A[2][NA], B[2][NE];
  la += 512 * A0; lb -= 512 * (E + NE - 1);
  auto ld = [&](int bu, u32 la_, u32 lb_) {
#pragma unroll
    for (int e = 0; e < NE; ++e) B[bu][e] = lds_b64x2(lb_ + 512 * (NE - 1 - e));
#pragma unroll
    for (int a = 0; a < NA; ++a) A[bu][a] = lds_b128(la_ + 512 * a);
  };
  auto mm = [&](int bu) {
#pragma unroll
    for (int e = 0; e < NE; ++e)
#pragma unroll
      for (int a = 0; a < NA; ++a)
        if (E + e + A0 + a <= 7) acc[E + e + A0 + a] = mfma16(A[bu][a], B[bu][e], acc[E + e + A0 + a]);
  };
  auto il = [&]() {
#pragma unroll
    for (int i = 0; i < NM; ++i) {
      __builtin_amdgcn_sched_group_barrier(0x008, 1, 0);
      __builtin_amdgcn_sched_group_barrier(0x100, (NL + NM - 1) / NM, 0);
    }
    SCHED0();
  };
  ld(0, la, lb);
  SCHED0();
#pragma unroll 1
  for (int qp = 0; qp < 3; ++qp) {
    ld(1, la - 64, lb - 64);
    mm(0);
    il();
    ld(0, la - 128, lb - 128);
    mm(1);
    il();
    la -= 128; lb -= 128;
  }
  ld(1, la - 64, lb - 64);
  mm(0);
  il();
  mm(1);
}

template <int G, int NCH>
__device__ __forceinline__ void k0_loader(
    const u16* __restrict__ Kin, const u16* __restrict__ Vin, const u16* __restrict__ shk,
    const u16* __restrict__ cvec, const u16* __restrict__ wmod, float* __restrict__ modp,
    const u16* __restrict__ Xpf, const u16* __restrict__ wpf, int lw, int lane, int pr, int xcd, int idx, u32 lbase
    ) {
  constexpr int TOP = G ? 2047 : 895, S0 = G ? 768 : 0;
  constexpr int NW = G ? 8 : 16;
  const int d0 = pr * 8;
  const size_t ro = (size_t)(S0 + 128 * lw + 2 * lane) * D_ + d0;
  const u16* kp = Kin + ro;
  const u16* vp = Vin + ro;
  u32x4 L[4 * NCH], wq[NW], cq[NW / 8];
  auto ld = [&](int n) {
    const u16* src = ((n & 2) ? vp : kp) + (size_t)(n >> 2) * 512 * D_ + (n & 1) * D_;
    L[n] = ldnt<u32x4>(src);
  };
  const int wrow = G ? (pr & 3) * 256 + lw * 64 + (lane >> 3) * 8 : (pr & 1) * 512 + lw * 128 + (lane >> 3) * 16;
  const int wcol = G ? 4096 + (pr >> 2) * 64 : (pr >> 1) * 64;
  const u16* wp = wmod + (size_t)wrow * 6144 + wcol + (lane & 7) * 8;
  auto item = [&](int n) {
    if (n < 4 * NCH) ld(n);
    else if (n < 4 * NCH + NW / 8) {
#pragma unroll
      for (int i = 8 * (n - 4 * NCH); i < 8 * (n - 4 * NCH) + 8; ++i) wq[i] = ldnt<u32x4>(wp + (size_t)i * 6144);
    }
  };
  u32x4 hk[NCH][3];
#pragma unroll
  for (int j = 0; j < NCH; ++j)
#pragma unroll
    for (int r = 0; r < 3; ++r) {
      const u32 row = (!G && j == 0 && lw == 0) ? 0u : (u32)(S0 + 512 * j + 128 * lw - 3 + r);
      hk[j][r] = *(const u32x4*)(Kin + (size_t)row * D_ + d0);
    }
  u32x4 sk[4];
#pragma unroll
  for (int i = 0; i < 4; ++i) sk[i] = *(const u32x4*)(shk + d0 * 4 + 8 * i);
#pragma unroll
  for (int i = 0; i < NW / 8; ++i) cq[i] = *(const u32x4*)(cvec + wrow + 8 * i);
  SCHED0();
  __builtin_amdgcn_s_setprio(K0_PRIO);
  __builtin_amdgcn_s_barrier();
  SCHED0();
#pragma unroll
  for (int n = 0; n < 4; ++n) item(n);
  u32 pf = 0;
  if (K0_PFB == 1 && lw < 2) {
    const int r = idx * 32 + lw * 16 + (lane >> 2);
    const int row = r < 256 ? 512 + r : 1024 + r;
    pf = ldnt<u32>(((lane & 2) ? Vin : Kin) + (size_t)row * D_ + xcd * 128 + (lane & 1) * 64);
  }
  item(4); item(5);
  SCHED0();
  if (!G) {
#pragma unroll
    for (int r = 0; r < 3; ++r) {
      asm volatile("" : "+s"(hk[0][r]));
      if (lw == 0) hk[0][r] = (u32x4){0u, 0u, 0u, 0u};
    }
  }
  const u32 uw = lbase + C_R1 + (TOP - 1 - S0 - 128 * lw - 2 * lane) * 2;
#pragma unroll
  for (int j = 0; j < NCH; ++j) {
#pragma unroll
    for (int hp = 0; hp < 4; ++hp) {
      const f32x2 W0 = {bf_lo(sk[hp][0]), bf_lo(sk[hp][2])}, W1 = {bf_hi(sk[hp][0]), bf_hi(sk[hp][2])};
      const f32x2 W2 = {bf_lo(sk[hp][1]), bf_lo(sk[hp][3])}, W3 = {bf_hi(sk[hp][1]), bf_hi(sk[hp][3])};
      const u32 kA = L[4 * j][hp], kB = L[4 * j + 1][hp];
      const u32 kB1 = shr1(hk[j][2][hp], kB);
      const u32 kA1 = shr1(hk[j][1][hp], kA);
      const u32 kB2 = shr1(hk[j][0][hp], kB1);
      const f32x2 fA = unpk(kA), fB = unpk(kB), fB1 = unpk(kB1), fA1 = unpk(kA1), fB2 = unpk(kB2);
      f32x2 a0 = fB2 * W3, a1 = fA1 * W3;
      a0 = __builtin_elementwise_fma(fA1, W2, a0); a1 = __builtin_elementwise_fma(fB1, W2, a1);
      a0 = __builtin_elementwise_fma(fB1, W1, a0); a1 = __builtin_elementwise_fma(fA, W1, a1);
      a0 = __builtin_elementwise_fma(fA, W0, a0);  a1 = __builtin_elementwise_fma(fB, W0, a1);
      a0 *= unpk(L[4 * j + 2][hp]);
      a1 *= unpk(L[4 * j + 3][hp]);
#pragma unroll
      for (int c = 0; c < 2; ++c) {
        const u32 pk = pk_bf(a1[c], a0[c]);
        const u32 o = uw + (2 * hp + c) * C_CH - j * 1024;
        *(LDSP u32*)(o) = pk;
        *(LDSP u32*)(o + (C_R0 - C_R1) - 4) = pk;
      }
    }
    if (lane == 0) lds_inc(lbase + C_FL + 4 * (G ? j : (j == 0 && NCH == 2 ? 0 : 1)));
    SCHED0();
    STAMP(2 + j);
#pragma unroll
    for (int n = 4 * j + 6; n < 4 * j + 10; ++n) item(n);
    SCHED0();
  }
  u32 pf2 = *(const u32*)(Xpf + (size_t)blockIdx.x * 8192 + (lw * 64 + lane) * 32);
  u32 sc[NW / 2];
#pragma unroll
  for (int i = 0; i < NW / 2; ++i) {
    u32 cv = cq[i >> 2][i & 3];
    sc[i] = pk_bf(silu_f(bf_lo(cv)), silu_f(bf_hi(cv)));
  }
  float ga[8];
#pragma unroll
  for (int j = 0; j < 8; ++j) ga[j] = 0.f;
#pragma unroll
  for (int i = 0; i < NW / 2; ++i) {
#pragma unroll
    for (int j = 0; j < 4; ++j) {
      u32 a = wq[2 * i][j], bb = wq[2 * i + 1][j];
      u32 lo = __builtin_amdgcn_perm(bb, a, 0x05040100u);
      u32 hi = __builtin_amdgcn_perm(bb, a, 0x07060302u);
      ga[2 * j] = __builtin_amdgcn_fdot2_f32_bf16(__builtin_bit_cast(bf16x2, lo), __builtin_bit_cast(bf16x2, sc[i]), ga[2 * j], false);
      ga[2 * j + 1] = __builtin_amdgcn_fdot2_f32_bf16(__builtin_bit_cast(bf16x2, hi), __builtin_bit_cast(bf16x2, sc[i]), ga[2 * j + 1], false);
    }
  }
  STAMP(5);
  u32 pf3[K0_WPF + 1];
  SCHED0();
#pragma unroll
  for (int j = 0; j < K0_WPF; ++j)
    pf3[j] = *(const u32*)(wpf + (size_t)(blockIdx.x + 256 * j) * F_ + ((lw & 1) * 56 + (lane < 56 ? lane : 55)) * 32);
  SCHED0();
#pragma unroll
  for (int j = 0; j < 8; ++j)
    ga[j] += __uint_as_float(__builtin_amdgcn_update_dpp(0u, __float_as_uint(ga[j]), 0x118, 0xf, 0xf, true));
  if (lane & 8) {
    u32 o = lbase + C_GP + ((lw * 4 + (lane >> 4)) * 64 + (lane & 7) * 8) * 4;
    *(LDSP f32x4*)(o) = (f32x4){ga[0], ga[1], ga[2], ga[3]};
    *(LDSP f32x4*)(o + 16) = (f32x4){ga[4], ga[5], ga[6], ga[7]};
  }
  u32 old = 0;
  if (lane == 0)
    asm volatile("ds_add_rtn_u32 %0, %1, %2\n\ts_waitcnt lgkmcnt(0)" : "=v"(old) : "v"(lbase + C_FL + 32), "v"(1u) : "memory");
  if (__builtin_amdgcn_readfirstlane(old) == 3) {
    float s = 0.f;
#pragma unroll
    for (int i = 0; i < 16; ++i) s += *(const LDSP float*)(lbase + C_GP + (i * 64 + lane) * 4);
    modp[(G ? (pr & 3) : (pr & 1)) * 6144 + wcol + lane] = s;
  }
  asm volatile("" :: "v"(pf), "v"(pf2));
#pragma unroll
  for (int j = 0; j < K0_WPF; ++j) asm volatile("" :: "v"(pf3[j]));
}

template <int G>
__device__ __forceinline__ void k0_mfma(
    const u16* __restrict__ Kin, const u16* __restrict__ Vin, const u16* __restrict__ ssmk,
    const u16* __restrict__ ssmD, u16* __restrict__ yp, int w, int lane, int pr, int xcd, int idx, u32 lbase
    ) {
  constexpr int TOP = G ? 2047 : 895, S0 = G ? 768 : 0;
  constexpr int NF = G ? 3 : 4;
  u32 pf = 0;
  const u16* pfp;
  {
    const int r = idx * 32 + (w & 1) * 16 + (lane >> 2);
    const int row = (w & 2) ? (r < 256 ? 512 + r : 1024 + r) : (r < 512 ? r : 256 + r);
    pfp = ((lane & 2) ? Vin : Kin) + (size_t)row * D_ + xcd * 128 + (lane & 1) * 64;
  }
  if (w < 2) pf = ldnt<u32>(pfp);
  const u16* fk = ssmk + (size_t)(pr * 8 + w) * S_;
  u32x4 fc[NF];
#pragma unroll
  for (int i = 0; i < NF; ++i) fc[i] = ldnt<u32x4>(fk + (lane + 64 * i) * 8);
  if (K0_PFB == 0 && (w >> 1) == 1) pf = ldnt<u32>(pfp);
  const u32 dD = *(const u32*)(ssmD + pr * 8 + (w & 6));
  SCHED0();
  const u32 cb = lbase + w * C_CH;
  constexpr int Z = (TOP - S0 + 1) * 2;
  if (lane < 56) *(LDSP u32x4*)(cb + (lane / 28) * 4576 + (lane % 28) * 16) = (u32x4){0u, 0u, 0u, 0u};
  if (lane < 16) { if ((lane & 7) < 5) *(LDSP u32x4*)(cb + ((lane >> 3) ? C_R0 : C_R1) + Z - 16 + (lane & 7) * 16) = (u32x4){0u, 0u, 0u, 0u}; }
  else if (!G && lane < 32) *(LDSP u32x4*)(cb + ((lane & 8) ? C_R0 : C_R1) + 128 + (lane & 7) * 16) = (u32x4){0u, 0u, 0u, 0u};
  if (w == 0 && lane >= 48) *(LDSP u32*)(lbase + C_FL + 4 * (lane - 48)) = 0u;
  BARRIER();
  const float Dv = (w & 1) ? bf_hi(dD) : bf_lo(dD);
#pragma unroll
  for (int i = 0; i < NF; ++i) {
    const int z = lane + 64 * i;
    const u32 prev = shr1(i ? __builtin_amdgcn_readlane(fc[i ? i - 1 : 0][3], 63) : 0u, fc[i][3]);
    u32x4 e;
    e[0] = __builtin_amdgcn_alignbit(fc[i][0], prev, 16);
    e[1] = __builtin_amdgcn_alignbit(fc[i][1], fc[i][0], 16);
    e[2] = __builtin_amdgcn_alignbit(fc[i][2], fc[i][1], 16);
    e[3] = __builtin_amdgcn_alignbit(fc[i][3], fc[i][2], 16);
    *(LDSP u32x4*)(cb + C_KE + z * 16) = e;
    *(LDSP u32x4*)(cb + C_KO + z * 16) = fc[i];
  }
  const int g = lane >> 4, i16 = lane & 15;
  const u32 la = cb + ((i16 & 1) ? C_KO : C_KE) + 64 * (i16 >> 1) + 16 * g;
  const u32 lb = cb + ((i16 & 1) ? C_R1 : C_R0) + 2 * (TOP - 3) - 8 * (i16 >> 1) + 16 * g;
  f32x4 acc[8];
#pragma unroll
  for (int c = 0; c < 8; ++c) acc[c] = (f32x4){0.f, 0.f, 0.f, 0.f};
  const u32 ur = cb + C_R1 + (TOP - 1 - 64 * g - 2 * i16) * 2;
  const u32 yw = lbase + C_Y + w * 4096 + (64 * g + 2 * i16) * 2;
  const u32 sel = (lane & 1) ? 0x07060302u : 0x05040100u;
  auto finA = [&](int c0, int c1, int k) {
#pragma unroll
    for (int c = c0; c < c1; ++c) {
      if (G || c < 3) {
        const u32 u01 = *(const LDSP u32*)(ur - 512 * c);
        const u32 u23 = *(const LDSP u32*)(ur - 512 * c - 64);
        *(LDSP u32*)(yw + 512 * c) = pk_bf(fmaf(bf_hi(u01), Dv, acc[c][0]), fmaf(bf_lo(u01), Dv, acc[c][1]));
        *(LDSP u32*)(yw + 512 * c + 64) = pk_bf(fmaf(bf_hi(u23), Dv, acc[c][2]), fmaf(bf_lo(u23), Dv, acc[c][3]));
      } else {
        *(LDSP u32*)(yw + 512 * c) = pk_bf(acc[c][0], acc[c][1]);
        *(LDSP u32*)(yw + 512 * c + 64) = pk_bf(acc[c][2], acc[c][3]);
      }
    }
    if (lane == 0) lds_inc(lbase + C_FL + 16 + 4 * k);
  };
  auto finB = [&](int r0) {
    const int r = r0 + lane;
    u32 y[8];
#pragma unroll
    for (int ch = 0; ch < 8; ++ch) y[ch] = *(const LDSP u32*)(lbase + C_Y + ch * 4096 + (r & ~1) * 2);
    u32x4 o = {__builtin_amdgcn_perm(y[1], y[0], sel), __builtin_amdgcn_perm(y[3], y[2], sel),
               __builtin_amdgcn_perm(y[5], y[4], sel), __builtin_amdgcn_perm(y[7], y[6], sel)};
    st16(yp + (G ? S_ * D_ - 768 * D_ : 0) + ((size_t)(r >> 3) * 128 + pr) * 64 + (r & 7) * 8, o);
  };
  asm volatile("" :: "v"(pf));
  STAMP(2);
  if (!G) {
    lds_wait(lbase + C_FL + 0, 2);
    STAMP(3);
    conv_stage<0, 1, 0, 8>(acc, la, lb);
    finA(0, 1, 0);
    lds_wait(lbase + C_FL + 4, 4);
    STAMP(4);
    {
      const bf16x8 B = lds_b64x2(lb - 64 * 24);
#pragma unroll
      for (int a = 0; a < 5; ++a) acc[3 + a] = mfma16(lds_b128(la + 512 * a), B, acc[3 + a]);
    }
    conv_stage<1, 2, 0, 3>(acc, la, lb);
    finA(1, 4, 1);
    conv_stage<1, 2, 3, 4>(acc, la, lb);
    STAMP(5);
    finA(4, 8, 2);
    lds_wait(lbase + C_FL + 16, 8);
    lds_wait(lbase + C_FL + 20, 8);
    finB(128 * w); finB(128 * w + 64);
    lds_wait(lbase + C_FL + 24, 8);
    finB(1024 + 128 * w); finB(1024 + 128 * w + 64);
  } else {
    lds_wait(lbase + C_FL + 0, 4);
    STAMP(3);
    conv_stage<3, 2, 0, 5>(acc, la, lb);
    finA(3, 5, 0);
    lds_wait(lbase + C_FL + 4, 4);
    STAMP(4);
    conv_stage<5, 2, 0, 3>(acc, la, lb);
    finA(5, 7, 1);
    bf16x8 A[8];
#pragma unroll
    for (int q = 0; q < 8; ++q) A[q] = lds_b128(la - 64 * q);
    lds_wait(lbase + C_FL + 16, 8);
    finB(768 + 64 * w);
    lds_wait(lbase + C_FL + 8, 2);
    STAMP(5);
    {
      bf16x8 B[8];
#pragma unroll
      for (int q = 0; q < 8; ++q) B[q] = lds_b64x2(lb - 64 * (56 + q));
      f32x4 t2 = {0.f, 0.f, 0.f, 0.f};
#pragma unroll
      for (int q = 0; q < 8; q += 2) { acc[7] = mfma16(A[q], B[q], acc[7]); t2 = mfma16(A[q + 1], B[q + 1], t2); }
      acc[7] += t2;
    }
    finA(7, 8, 2);
    lds_wait(lbase + C_FL + 20, 8);
    finB(1280 + 64 * w);
    if (w < 4) {
      lds_wait(lbase + C_FL + 24, 8);
      finB(1792 + 64 * w);
    }
  }
}

#define ST_ARG
__global__ __launch_bounds__(768, 3) void k0_conv(
    const u16* __restrict__ Kin, const u16* __restrict__ Vin, const u16* __restrict__ shk,
    const u16* __restrict__ ssmk, const u16* __restrict__ ssmD, const u16* __restrict__ cvec,
    const u16* __restrict__ wmod, float* __restrict__ modp, u16* __restrict__ yp,
    const u16* __restrict__ Xpf, const u16* __restrict__ w1pf, const u16* __restrict__ w3pf) {
  extern __shared__ __attribute__((aligned(16))) char smem[];
  STAMP_DECL
  const int t = threadIdx.x, lane = t & 63;
  const int w = __builtin_amdgcn_readfirstlane(t >> 6);
  const int b = blockIdx.x, xcd = b & 7, idx = b >> 3;
  const int pr = xcd * 16 + (idx >> 1);
  const u32 lbase = (u32)(uintptr_t)(LDSP char*)smem;
  if (idx & 1) {
    if (w < 8) k0_mfma<1>(Kin, Vin, ssmk, ssmD, yp, w, lane, pr, xcd, idx, lbase ST_ARG);
    else if (w < 10) k0_loader<1, 3>(Kin, Vin, shk, cvec, wmod, modp, Xpf, w1pf, w - 8, lane, pr, xcd, idx, lbase ST_ARG);
    else k0_loader<1, 2>(Kin, Vin, shk, cvec, wmod, modp, Xpf, w3pf, w - 8, lane, pr, xcd, idx, lbase ST_ARG);
  } else {
    if (w < 8) k0_mfma<0>(Kin, Vin, ssmk, ssmD, yp, w, lane, pr, xcd, idx, lbase ST_ARG);
    else if (w < 10) k0_loader<0, 2>(Kin, Vin, shk, cvec, wmod, modp, Xpf, w1pf, w - 8, lane, pr, xcd, idx, lbase ST_ARG);
    else k0_loader<0, 1>(Kin, Vin, shk, cvec, wmod, modp, Xpf, w3pf, w - 8, lane, pr, xcd, idx, lbase ST_ARG);
  }
  STAMP_END(0);
}

__device__ __forceinline__ float wave_sum(float x) {
  x += __uint_as_float(__builtin_amdgcn_update_dpp(0u, __float_as_uint(x), 0x111, 0xf, 0xf, true));
  x += __uint_as_float(__builtin_amdgcn_update_dpp(0u, __float_as_uint(x), 0x112, 0xf, 0xf, true));
  x += __uint_as_float(__builtin_amdgcn_update_dpp(0u, __float_as_uint(x), 0x114, 0xf, 0xf, true));
  x += __uint_as_float(__builtin_amdgcn_update_dpp(0u, __float_as_uint(x), 0x118, 0xf, 0xf, true));
  x += __uint_as_float(__builtin_amdgcn_update_dpp(0u, __float_as_uint(x), 0x142, 0xa, 0xf, true));
  x += __uint_as_float(__builtin_amdgcn_update_dpp(0u, __float_as_uint(x), 0x143, 0xc, 0xf, true));
  return __uint_as_float(__builtin_amdgcn_readlane(__float_as_uint(x), 63));
}

constexpr int N_YRS = 2048 + 32;
constexpr int N_MOD = 8 * N_YRS;
constexpr int N_H = N_MOD + 10240;
constexpr int N_LDS = N_H + 16384 + 256;
#define K1_PFN 256
#define K1_PFW 0
#define K1_PF (K1_PFW ? 1 : 0)

__global__ __launch_bounds__(512) void k1_norm(
    const u16* __restrict__ X, const u16* __restrict__ yp, const float* __restrict__ modp,
    u16* __restrict__ x1out, u16* __restrict__ h2out,
    const u16* __restrict__ w1pf, const u16* __restrict__ w3pf, u16* __restrict__ gbf, char* __restrict__ xb) {
  __shared__ __attribute__((aligned(16))) char smem[N_LDS];
  const int t = threadIdx.x, lane = t & 63;
  const int w = __builtin_amdgcn_readfirstlane(t >> 6);
  STAMP_DECL
  const int rb = blockIdx.x;
  const int row = rb * 8 + w;
  const u32 lbase = (u32)(uintptr_t)(LDSP char*)smem;
  u32x4 xq[2], yq[2], yq1[2], mp[8];
  const bool two = rb >= 96;
  {
    const u16* xrow = X + (size_t)row * D_;
    gld16(xq[0], xrow, lane * 16);
    gld16(xq[1], xrow, lane * 16 + 1024);
    const u16* y0 = yp + (size_t)rb * 8192;
    const u16* y1 = yp + (size_t)(two ? rb + 160 : rb) * 8192;
    gld16(yq[0], y0, t * 16);
    gld16(yq[1], y0, t * 16 + 8192);
    gld16(yq1[0], y1, t * 16);
    gld16(yq1[1], y1, t * 16 + 8192);
#pragma unroll
    for (int i = 0; i < 2; ++i) {
      gld16(mp[2 * i], modp, t * 16 + i * 8192);
      gld16(mp[2 * i + 1], modp, t * 16 + i * 8192 + 24576);
    }
#pragma unroll
    for (int h = 0; h < 4; ++h) gld16(mp[4 + h], modp, t * 16 + 16384 + h * 24576);
    SCHED0();
    __builtin_amdgcn_s_barrier();
    SCHED0();
#pragma unroll
    for (int j = 0; j < K1_PF; ++j)
      glds4(((w & 2) ? w3pf : w1pf) + (size_t)(rb + 256 * (((w >> 2) + K0_WPF) & 3)) * F_,
            (w < K1_PFW && rb < K1_PFN) ? (u32)((w & 1) * 56 + (lane < 56 ? lane : 55)) * 64u : 0u, lbase + N_LDS - 256);
#pragma unroll
    for (int i = 0; i < 2; ++i) st16(xb + (size_t)rb * 16384 + i * 8192 + t * 16, (u32x4){0u, 0u, 0u, 0u});
  }
  asm volatile("s_waitcnt vmcnt(%2)" : "+v"(xq[0]), "+v"(xq[1]) : "n"(14 + K1_PF) : "memory");
  float x[16];
#pragma unroll
  for (int i = 0; i < 8; ++i) {
    u32 v = xq[i >> 2][i & 3];
    x[2 * i] = bf_lo(v); x[2 * i + 1] = bf_hi(v);
  }
  float s = 0.f;
#pragma unroll
  for (int i = 0; i < 16; ++i) s += x[i];
  const float mean = wave_sum(s) * (1.0f / 1024.0f);
  float q = 0.f;
#pragma unroll
  for (int i = 0; i < 16; ++i) { float d = x[i] - mean; q = fmaf(d, d, q); }
  const float var = wave_sum(q) * (1.0f / 1024.0f);
  const float rstd = __builtin_amdgcn_rsqf(var + 1e-5f);
  asm volatile("s_waitcnt vmcnt(%4)" : "+v"(yq[0]), "+v"(yq[1]), "+v"(yq1[0]), "+v"(yq1[1]) : "n"(10 + K1_PF) : "memory");
#pragma unroll
  for (int i = 0; i < 2; ++i) {
    if (two) {
#pragma unroll
      for (int e = 0; e < 4; ++e) {
        const f32x2 sm = unpk(yq[i][e]) + unpk(yq1[i][e]);
        yq[i][e] = pk_bf(sm[0], sm[1]);
      }
    }
    *(LDSP u32x4*)(lbase + (t & 7) * N_YRS + ((t >> 3) + 64 * i) * 16) = yq[i];
  }
  asm volatile("s_waitcnt vmcnt(%8)" : "+v"(mp[0]), "+v"(mp[1]), "+v"(mp[2]), "+v"(mp[3]), "+v"(mp[4]), "+v"(mp[5]),
               "+v"(mp[6]), "+v"(mp[7]) : "n"(2 + K1_PF) : "memory");
#pragma unroll
  for (int i = 0; i < 3; ++i) {
    f32x4 sv = __builtin_bit_cast(f32x4, mp[2 * i]) + __builtin_bit_cast(f32x4, mp[2 * i + 1]);
    if (i == 2) sv += __builtin_bit_cast(f32x4, mp[6]) + __builtin_bit_cast(f32x4, mp[7]);
    const u32x2 pv = (u32x2){pk_bf(sv[0], sv[1]), pk_bf(sv[2], sv[3])};
    if (i < 2 || w < 4) *(LDSP u32x2*)(lbase + N_MOD + (t + 512 * i) * 8) = pv;
    else if (rb == 0) st8(gbf + (t - 256) * 4, pv);
  }
  STAMP(2);
  BARRIER();
  STAMP(3);
  u32x4 yv[2], mq[5][2];
#pragma unroll
  for (int hf = 0; hf < 2; ++hf) yv[hf] = *(const LDSP u32x4*)(lbase + w * N_YRS + hf * 1024 + lane * 16);
#pragma unroll
  for (int m = 0; m < 5; ++m)
#pragma unroll
    for (int hf = 0; hf < 2; ++hf) mq[m][hf] = *(const LDSP u32x4*)(lbase + N_MOD + m * 2048 + hf * 1024 + lane * 16);
  float x1[16];
#pragma unroll
  for (int i = 0; i < 16; ++i) {
    const int hf = i >> 3, e = i & 7;
    u32 sa = mq[0][hf][e >> 1], ca = mq[1][hf][e >> 1], ga = mq[2][hf][e >> 1], yy = yv[hf][e >> 1];
    float saf = (e & 1) ? bf_hi(sa) : bf_lo(sa);
    float caf = (e & 1) ? bf_hi(ca) : bf_lo(ca);
    float gaf = (e & 1) ? bf_hi(ga) : bf_lo(ga);
    float yf = (e & 1) ? bf_hi(yy) : bf_lo(yy);
    float h = fmaf((x[i] - mean) * rstd, 1.0f + caf, saf);
    x1[i] = fmaf(gaf, h * yf, x[i]);
  }
  u32x4 xo[2];
#pragma unroll
  for (int i = 0; i < 8; ++i) {
    u32 p = pk_bf(x1[2 * i], x1[2 * i + 1]);
    xo[i >> 2][i & 3] = p;
    x1[2 * i] = bf_lo(p); x1[2 * i + 1] = bf_hi(p);
  }
#pragma unroll
  for (int hf = 0; hf < 2; ++hf) st16(x1out + (size_t)row * D_ + hf * 512 + lane * 8, xo[hf]);
  float s2 = 0.f;
#pragma unroll
  for (int i = 0; i < 16; ++i) s2 += x1[i];
  const float mean2 = wave_sum(s2) * (1.0f / 1024.0f);
  float q2 = 0.f;
#pragma unroll
  for (int i = 0; i < 16; ++i) { float d = x1[i] - mean2; q2 = fmaf(d, d, q2); }
  const float var2 = wave_sum(q2) * (1.0f / 1024.0f);
  const float rstd2 = __builtin_amdgcn_rsqf(var2 + 1e-5f);
#pragma unroll
  for (int hf = 0; hf < 2; ++hf) {
    u32x4 ho;
#pragma unroll
    for (int e = 0; e < 4; ++e) {
      const int i = hf * 4 + e;
      u32 sb = mq[3][hf][e], cb = mq[4][hf][e];
      float h0 = fmaf((x1[2 * i] - mean2) * rstd2, 1.0f + bf_lo(cb), bf_lo(sb));
      float h1 = fmaf((x1[2 * i + 1] - mean2) * rstd2, 1.0f + bf_hi(cb), bf_hi(sb));
      ho[e] = pk_bf(h0, h1);
    }
    *(LDSP u32x4*)(lbase + N_H + (hf * 64 + lane) * 128 + ((w ^ (lane >> 1)) & 7) * 16) = ho;
  }
  STAMP(4);
  WAIT_VM(4);
  BARRIER();
#pragma unroll
  for (int i = 0; i < 2; ++i) {
    const int chunk = (t >> 3) + 64 * i, r = t & 7;
    u32x4 v = *(const LDSP u32x4*)(lbase + N_H + chunk * 128 + ((r ^ (chunk >> 1)) & 7) * 16);
    st16((char*)h2out + (size_t)(rb >> 1) * 32768 + chunk * 256 + (rb & 1) * 128 + r * 16, v);
  }
  STAMP_END(1);
}

constexpr int G1_R = 5;
constexpr int G1_WOFF = 16384;
constexpr int G1_UP = 7168 + 16;
constexpr int G1_SLOT = 16384 + G1_UP + 7168;
constexpr int G1_NK = 32;

struct G1Frags { bf16x8 x[4]; bf16x8 wg[3], wu[3], wm; };

__global__ __launch_bounds__(512, 2) void k2_gemm1(
    const u16* __restrict__ h2, const u16* __restrict__ w1, const u16* __restrict__ w3,
    u16* __restrict__ hh, const u16* __restrict__ w2pf) {
  extern __shared__ __attribute__((aligned(16))) char smem[];
  const int t = threadIdx.x, lane = t & 63;
  const int w = __builtin_amdgcn_readfirstlane(t >> 6);
  const int b = blockIdx.x, xcd = b & 7, idx = b >> 3;
  STAMP_DECL
  const int mblk = (xcd & 1) * 4 + (idx & 3), nblk = (xcd >> 1) * 8 + (idx >> 2);
  const int wm = w & 3, wn = w >> 2;
  const u32 lbase = (u32)(uintptr_t)(LDSP char*)smem;

  const char* xsrc = (const char*)h2 + (size_t)(mblk * 16 + 2 * w) * 32768;
  const char* wsrc = (const char*)((w >> 2) ? w3 : w1) + nblk * 224;
  const u32 xv0 = lane * 16, xv1 = lane * 16 + 32768;
  u32 wv0, wv1;
  {
    int c0 = lane, c1 = lane + 64;
    if (c1 > 111) c1 = 111;
    int p0 = 8 * (w & 3) + c0 / 14, p1 = 8 * (w & 3) + c1 / 14;
    int k0 = 8 * ((p0 >> 2) & 3) + 4 * (p0 >> 4) + (p0 & 3);
    int k1 = 8 * ((p1 >> 2) & 3) + 4 * (p1 >> 4) + (p1 & 3);
    wv0 = k0 * 7168 + (c0 % 14) * 16;
    wv1 = k1 * 7168 + (c1 % 14) * 16;
  }
  const u32 xdst = lbase + 2 * w * 1024;
  const u32 wdst = lbase + G1_WOFF + (w >> 2) * G1_UP + (w & 3) * 1792;
  auto issue = [&](int kb, u32 boff) {
    const char* xs = xsrc + kb * 1024;
    const char* ws = wsrc + (size_t)kb * (32 * 7168);
    glds16(xs, xv0, xdst + boff);
    glds16(xs, xv1, xdst + boff + 1024);
    glds16(ws, wv0, wdst + boff);
    glds16_48(ws, wv1, wdst + boff + 1024);
  };
  const int g = lane >> 4, q = (lane >> 2) & 3, p = lane & 3;
  const u32 xr = lbase + wm * 4096 + lane * 16;
  const u32 wr = lbase + G1_WOFF + (4 * g + q) * 224 + 8 * p + wn * 128;
  const u32 wrm = lbase + G1_WOFF + (4 * g + q) * 224 + (p & 1) * G1_UP + (48 + 8 * wn + 4 * (p >> 1)) * 2;
  auto rd = [&](G1Frags& f, u32 boff) {
#pragma unroll
    for (int i = 0; i < 4; ++i) f.x[i] = lds_b128(xr + boff + i * 1024);
#pragma unroll
    for (int i = 0; i < 3; ++i) {
      f.wg[i] = lds_tr2(wr + boff + 32 * i, wr + boff + 32 * i + 3584);
      f.wu[i] = lds_tr2(wr + boff + G1_UP + 32 * i, wr + boff + G1_UP + 32 * i + 3584);
    }
    f.wm = lds_tr2(wrm + boff, wrm + boff + 3584);
  };
  f32x4 ag[4][3], au[4][3], am[4];
#pragma unroll
  for (int m = 0; m < 4; ++m) {
#pragma unroll
    for (int i = 0; i < 3; ++i) { ag[m][i] = (f32x4){0.f, 0.f, 0.f, 0.f}; au[m][i] = (f32x4){0.f, 0.f, 0.f, 0.f}; }
    am[m] = (f32x4){0.f, 0.f, 0.f, 0.f};
  }
  auto mm = [&](const G1Frags& f) {
#pragma unroll
    for (int m = 0; m < 4; ++m) {
#pragma unroll
      for (int i = 0; i < 3; ++i) {
        ag[m][i] = mfma16(f.wg[i], f.x[m], ag[m][i]);
        au[m][i] = mfma16(f.wu[i], f.x[m], au[m][i]);
      }
      am[m] = mfma16(f.wm, f.x[m], am[m]);
    }
  };

  G1Frags fa, fb;
  issue(0, 0);
  SCHED0();
  __builtin_amdgcn_s_barrier();
  SCHED0();
  issue(1, G1_SLOT);
  SCHED0();
  __builtin_amdgcn_s_barrier();
  SCHED0();
  issue(2, 2 * G1_SLOT);
  STAMP(2);
  WAIT_VM(8);
  BARRIER();
  STAMP(3);
  rd(fa, 0);
  if (wn) __builtin_amdgcn_s_setprio(1);
  u32 cur = 0;
  auto nxt = [&](u32 o) { u32 n = o + G1_SLOT; return n == G1_R * G1_SLOT ? 0u : n; };
  auto il = [&]() {
#pragma unroll
    for (int i = 0; i < 18; ++i) {
      __builtin_amdgcn_sched_group_barrier(0x008, 1, 0);
      __builtin_amdgcn_sched_group_barrier(0x100, 1, 0);
    }
    __builtin_amdgcn_sched_group_barrier(0x008, 10, 0);
    SCHED0();
  };
  auto step = [&](G1Frags& fc, G1Frags& fn, int kb, u32 bc, u32 bn) {
    if (!wn) issue(kb, bc);
    SCHED0();
    rd(fn, bn);
    mm(fc);
    il();
    if (wn) issue(kb, bc);
    SCHED0();
  };
  auto step0 = [&](G1Frags& fc, G1Frags& fn, u32 bn) {
    rd(fn, bn);
    mm(fc);
    il();
  };
  WAIT_VM(4); BARRIER();
  issue(3, 3 * G1_SLOT);
  step(fa, fb, 4, 4 * G1_SLOT, G1_SLOT);
  WAIT_VM(8); BARRIER();
  issue(5, 0);
  step(fb, fa, 6, G1_SLOT, 2 * G1_SLOT);
  glds4(w2pf, (u32)((b * 8 + w) * 56 + (lane < 56 ? lane : 55)) * 64u, lbase + G1_R * G1_SLOT);
  cur = 2 * G1_SLOT;
#pragma unroll 1
  for (int tt = 2; tt < G1_NK - G1_R - 1; tt += 2) {
    u32 n1 = nxt(cur), n2 = nxt(n1);
    WAIT_VM(4 * (G1_R - 2)); BARRIER();
    step(fa, fb, tt + G1_R, cur, n1);
    WAIT_VM(4 * (G1_R - 2)); BARRIER();
    step(fb, fa, tt + 1 + G1_R, n1, n2);
    cur = n2;
  }
  STAMP(4);
  {
    u32 n1 = nxt(cur), n2 = nxt(n1), n3 = nxt(n2), n4 = nxt(n3);
    WAIT_VM(12); BARRIER();
    step(fa, fb, G1_NK - 1, cur, n1);
    WAIT_VM(12); BARRIER();
    step0(fb, fa, n2);
    WAIT_VM(8); BARRIER();
    step0(fa, fb, n3);
    WAIT_VM(4); BARRIER();
    step0(fb, fa, n4);
    WAIT_VM(0); BARRIER();
    step0(fa, fb, cur);
    mm(fb);
  }

  STAMP(5);
  const int m16 = lane & 15;
  char* hbase = (char*)hh + (size_t)(mblk * 16 + wm * 4) * (112 * 1024) + m16 * 16;
  const int c0 = nblk * 14 + wn * 8;
  auto swiglu4 = [&](const f32x4& gt, const f32x4& up) {
    float o[4];
#pragma unroll
    for (int r = 0; r < 4; ++r) o[r] = silu_f(gt[r]) * up[r];
    return (u32x2){pk_bf(o[0], o[1]), pk_bf(o[2], o[3])};
  };
#pragma unroll
  for (int mp = 0; mp < 2; ++mp) {
#pragma unroll
    for (int i = 0; i < 3; ++i) {
      u32x2 pa = swiglu4(ag[2 * mp][i], au[2 * mp][i]);
      u32x2 pb = swiglu4(ag[2 * mp + 1][i], au[2 * mp + 1][i]);
      auto s0 = __builtin_amdgcn_permlane16_swap(pa[0], pb[0], false, false);
      auto s1 = __builtin_amdgcn_permlane16_swap(pa[1], pb[1], false, false);
      st16(hbase + (size_t)(2 * mp + (g & 1)) * (112 * 1024) + (c0 + 2 * i + (g >> 1)) * 256,
           (u32x4){s0[0], s1[0], s0[1], s1[1]});
    }
  }
  {
    u32x2 qm[2];
#pragma unroll
    for (int mp = 0; mp < 2; ++mp) {
      f32x4 gt, up;
#pragma unroll
      for (int r = 0; r < 4; ++r) {
        auto sw = __builtin_amdgcn_permlane16_swap(__float_as_uint(am[2 * mp][r]), __float_as_uint(am[2 * mp + 1][r]), false, false);
        gt[r] = __uint_as_float(sw[0]); up[r] = __uint_as_float(sw[1]);
      }
      qm[mp] = swiglu4(gt, up);
    }
    auto s0 = __builtin_amdgcn_permlane32_swap(qm[0][0], qm[1][0], false, false);
    auto s1 = __builtin_amdgcn_permlane32_swap(qm[0][1], qm[1][1], false, false);
    st16(hbase + (size_t)g * (112 * 1024) + (nblk * 14 + 6 + wn) * 256, (u32x4){s0[0], s1[0], s0[1], s1[1]});
  }
  STAMP_END(2);
}

constexpr int G2_R = 6;
#define G2_T 6
#define G2_TP 2
#define G2_XQ 24
#define G2_PB 5
constexpr int G2_F = (56 - G2_T) / 2;
constexpr int G2_NT = G2_F + G2_T;
constexpr int G2_SLOT = 16384;
constexpr int G2_XO = G2_R * G2_SLOT;
constexpr int G2_LDS = G2_XO + 32768;
constexpr int g2_nis(int s) { return s >= G2_NT ? 0 : (s < G2_F ? 4 : 3); }
constexpr int g2_ext(int u) { return (u == G2_F ? 2 : 0) + (u == G2_XQ ? 4 : 0) + (u == G2_NT - G2_TP ? 2 : 0); }
constexpr int g2_wait(int t) {
  int n = 0;
  for (int s = t + 2; s <= t + G2_R - 1; ++s) n += g2_nis(s);
  for (int u = (t + 1 - G2_R > 0 ? t + 1 - G2_R : 0); u <= t - 1; ++u) n += g2_ext(u);
  return n;
}
struct G2W { bf16x8 w[8]; };
__global__ __launch_bounds__(512, 2) void k3_gemm2(
    const u16* __restrict__ hh, const u16* __restrict__ w2, const u16* __restrict__ x1,
    const u16* __restrict__ gbf, u16* __restrict__ out, char* __restrict__ xb) {
  extern __shared__ __attribute__((aligned(16))) char smem[];
  const int t = threadIdx.x, lane = t & 63;
  const int w = __builtin_amdgcn_readfirstlane(t >> 6);
  const int b = blockIdx.x, xcd = b & 7, idx = b >> 3;
  STAMP_DECL
  const int kh = xcd >> 2;
  const int mblk = (xcd & 1) * 8 + (idx & 7), nblk = ((xcd >> 1) & 1) * 4 + (idx >> 3);
  const int wm = w & 3, wk = w >> 2;
  const u32 lbase = (u32)(uintptr_t)(LDSP char*)smem;

  const char* xs0 = (const char*)hh + (size_t)(mblk * 8 + 2 * wm) * (112 * 1024) + wk * 1024;
  const char* ws0 = (const char*)w2 + nblk * 256;
  const char* xsf = xs0 + kh * (G2_NT * 2048);
  const char* wsf = ws0 + (size_t)kh * (G2_NT * 64 * 2048);
  const u32 xv0 = lane * 16, xv1 = lane * 16 + 112 * 1024;
  u32 wvk, wvs;
  {
    int pos = 8 * w + (lane >> 3);
    int pp = pos & 31;
    int k = (pos & 32) + 8 * ((pp >> 2) & 3) + 4 * (pp >> 4) + (pp & 3);
    int cc = (lane & 7) ^ (((pos >> 1) & 3) << 1);
    wvk = k * 2048 + cc * 16 + kh * 128;
    wvs = wvk ^ 128;
  }
  const u32 wdst = lbase + w * 1024;
  u32x4 xr[G2_R][2];
  auto issue_w = [&](int s, bool full, u32 boff) {
    const char* ws = (full ? wsf : ws0) + (size_t)s * (64 * 2048);
    glds16(ws, wvk, wdst + boff);
    if (full) glds16(ws, wvs, wdst + boff + 8192);
  };
  auto issue_x = [&](int s, bool full, u32x4 (&d)[2]) {
    const char* xs = (full ? xsf : xs0) + s * 2048;
    gld16(d[0], xs, xv0);
    gld16(d[1], xs, xv1);
  };
  const int g = lane >> 4, q = (lane >> 2) & 3, p = lane & 3;
  const int key = (2 * g + (q >> 1)) & 3;
  u32 wra[4];
#pragma unroll
  for (int nt = 0; nt < 4; ++nt)
    wra[nt] = lbase + wk * 4096 + (4 * g + q) * 128 + ((nt ^ key) * 32) + 8 * p;
  auto rd = [&](G2W& f, bool full, u32 boff) {
#pragma unroll
    for (int nt = 0; nt < 8; ++nt)
      if (nt < 4 || full)
        f.w[nt] = lds_tr2(wra[nt & 3] + boff + (nt >> 2) * 8192, wra[nt & 3] + boff + (nt >> 2) * 8192 + 2048);
  };
  f32x4 acc[2][8];
#pragma unroll
  for (int m = 0; m < 2; ++m)
#pragma unroll
    for (int nt = 0; nt < 8; ++nt) acc[m][nt] = (f32x4){0.f, 0.f, 0.f, 0.f};
  auto mm = [&](const G2W& f, bool full, const u32x4 (&x)[2]) {
#pragma unroll
    for (int nt = 0; nt < 8; ++nt)
      if (nt < 4 || full) {
#pragma unroll
        for (int m = 0; m < 2; ++m) acc[m][nt] = mfma16(f.w[nt], __builtin_bit_cast(bf16x8, x[m]), acc[m][nt]);
      }
  };
  auto il = [&](int nm, int nr) {
#pragma unroll
    for (int i = 0; i < 16; ++i) {
      if (i < nm) __builtin_amdgcn_sched_group_barrier(0x008, 1, 0);
      if (i < nr) __builtin_amdgcn_sched_group_barrier(0x100, 1, 0);
    }
    SCHED0();
  };
  const int m16 = lane & 15;
  const int row = mblk * 128 + (2 * wm + wk) * 16 + m16;
  const int col0 = nblk * 128 + kh * 64 + (g & 1) * 16 + (g >> 1) * 8;
  u32x4 xq[2], gq[2], pl[2];
  G2W fa, fb;
#pragma unroll
  for (int s = 0; s < G2_R; ++s) {
    issue_w(s, true, s * G2_SLOT);
    issue_x(s, true, xr[s]);
    if (s < G2_PB) {
      SCHED0();
      __builtin_amdgcn_s_barrier();
      SCHED0();
    }
  }
  STAMP(2);
  WAIT_VM(4 * (G2_R - 1));
  BARRIER();
  STAMP(3);
  rd(fa, true, 0);
#define G2_TIE(r) asm volatile("" : "+v"(xr[r][0]), "+v"(xr[r][1]))
#pragma unroll 1
  for (int tt = 0; tt < 18; tt += G2_R) {
#pragma unroll
    for (int r = 0; r < G2_R; ++r) {
      WAIT_VM(4 * (G2_R - 2)); BARRIER();
      G2_TIE(r);
      issue_w(tt + r + G2_R, true, r * G2_SLOT);
      SCHED0();
      rd((r & 1) ? fa : fb, true, ((r + 1) % G2_R) * G2_SLOT);
      mm((r & 1) ? fb : fa, true, xr[r]);
      il(16, 16);
      issue_x(tt + r + G2_R, true, xr[r]);
    }
  }
  STAMP(4);
  char* const xsend = xb + (size_t)(b ^ 4) * 16384 + w * 2048 + lane * 16;
  const char* const xrecv = xb + (size_t)b * 16384 + w * 2048;
#pragma unroll
  for (int tt = 18; tt < G2_NT; ++tt) {
    const int r = tt % G2_R;
    if (tt + 1 < G2_NT) { WAIT_VM(g2_wait(tt)); BARRIER(); }
    G2_TIE(r);
    if (tt + G2_R < G2_NT) issue_w(tt + G2_R, tt + G2_R < G2_F, r * G2_SLOT);
    f32x4 ex[4];
    if (tt == G2_F) {
#pragma unroll
      for (int nt = 0; nt < 4; ++nt) ex[nt] = *(const LDSP f32x4*)(lbase + G2_XO + ((w ^ 4) * 4 + nt) * 1024 + lane * 16);
    }
    SCHED0();
    if (tt + 1 < G2_NT) rd((r & 1) ? fa : fb, tt + 1 < G2_F, ((r + 1) % G2_R) * G2_SLOT);
    mm((r & 1) ? fb : fa, tt < G2_F, xr[r]);
    il(tt < G2_F ? 16 : 8, tt + 1 < G2_F ? 16 : (tt + 1 < G2_NT ? 8 : 0));
    if (tt + G2_R < G2_NT) issue_x(tt + G2_R, tt + G2_R < G2_F, xr[r]);
    if (tt == G2_F - 1) {
#pragma unroll
      for (int nt = 0; nt < 4; ++nt)
        *(LDSP f32x4*)(lbase + G2_XO + (w * 4 + nt) * 1024 + lane * 16) = wk ? acc[0][4 + nt] : acc[1][4 + nt];
    }
    if (tt == G2_F) {
#pragma unroll
      for (int j = 0; j < 2; ++j) {
        u32x4 pv;
#pragma unroll
        for (int h = 0; h < 2; ++h) {
          const f32x4 o = ex[2 * j + h] + (wk ? acc[1][4 + 2 * j + h] : acc[0][4 + 2 * j + h]);
          pv[2 * h] = __builtin_elementwise_max(pk_bf(o[0], o[1]), 1u);
          pv[2 * h + 1] = __builtin_elementwise_max(pk_bf(o[2], o[3]), 1u);
        }
        st16(xsend + j * 1024, pv);
      }
    }
    if (tt == G2_XQ) {
#pragma unroll
      for (int j = 0; j < 2; ++j) {
        gld16(xq[j], x1, (u32)(row * D_ + col0 + 32 * j) * 2u);
        gld16(gq[j], gbf, (u32)(col0 + 32 * j) * 2u);
      }
    }
    if (tt == G2_NT - G2_TP) {
#pragma unroll
      for (int j = 0; j < 2; ++j) gld16_sc1(pl[j], xrecv, lane * 16 + j * 1024);
    }
    SCHED0();
  }
  STAMP(5);
#pragma unroll
  for (int nt = 0; nt < 4; ++nt)
    *(LDSP f32x4*)(lbase + G2_XO + (w * 4 + nt) * 1024 + lane * 16) = wk ? acc[0][nt] : acc[1][nt];
  BARRIER();
  f32x4 o[4];
#pragma unroll
  for (int nt = 0; nt < 4; ++nt)
    o[nt] = *(const LDSP f32x4*)(lbase + G2_XO + ((w ^ 4) * 4 + nt) * 1024 + lane * 16) + (wk ? acc[1][nt] : acc[0][nt]);
  for (;;) {
    asm volatile("s_waitcnt vmcnt(0)" : "+v"(pl[0]), "+v"(pl[1]),
                 "+v"(xq[0]), "+v"(xq[1]), "+v"(gq[0]), "+v"(gq[1]) :: "memory");
    const u32x4 a = __builtin_elementwise_min(pl[0], pl[1]);
    const u32 ok = __builtin_elementwise_min(__builtin_elementwise_min(a[0], a[1]), __builtin_elementwise_min(a[2], a[3]));
    if (__builtin_amdgcn_ballot_w64(ok == 0u) == 0ull) break;
    __builtin_amdgcn_s_sleep(2);
#pragma unroll
    for (int j = 0; j < 2; ++j) gld16_sc1(pl[j], xrecv, lane * 16 + j * 1024);
  }
#pragma unroll
  for (int j = 0; j < 2; ++j) {
    u32 bq[2][2];
#pragma unroll
    for (int h = 0; h < 2; ++h) {
      const f32x4 v = o[2 * j + h] + (f32x4){bf_lo(pl[j][2 * h]), bf_hi(pl[j][2 * h]), bf_lo(pl[j][2 * h + 1]), bf_hi(pl[j][2 * h + 1])};
      bq[h][0] = pk_bf(v[0], v[1]); bq[h][1] = pk_bf(v[2], v[3]);
    }
    auto s0 = __builtin_amdgcn_permlane16_swap(bq[0][0], bq[1][0], false, false);
    auto s1 = __builtin_amdgcn_permlane16_swap(bq[0][1], bq[1][1], false, false);
    const u32x4 f = (u32x4){s0[0], s1[0], s0[1], s1[1]};
    u32x4 res;
#pragma unroll
    for (int e = 0; e < 4; ++e)
      res[e] = pk_bf(fmaf(bf_lo(gq[j][e]), bf_lo(f[e]), bf_lo(xq[j][e])), fmaf(bf_hi(gq[j][e]), bf_hi(f[e]), bf_hi(xq[j][e])));
    *(u32x4*)(out + (size_t)row * D_ + col0 + 32 * j) = res;
  }
  STAMP_END(3);
}

extern "C" void kernel_launch(void* const* d_in, const int* in_sizes, int n_in,
                              void* d_out, int out_size, void* d_ws, size_t ws_size,
                              hipStream_t stream) {
  const u16* x = (const u16*)d_in[0];
  const u16* kin = (const u16*)d_in[1];
  const u16* vin = (const u16*)d_in[2];
  const u16* shk = (const u16*)d_in[3];
  const u16* ssmk = (const u16*)d_in[4];
  const u16* ssmD = (const u16*)d_in[5];
  const u16* w1 = (const u16*)d_in[7];
  const u16* w3 = (const u16*)d_in[8];
  const u16* w2 = (const u16*)d_in[9];
  const u16* cvec = (const u16*)d_in[10];
  const u16* wmod = (const u16*)d_in[11];
  char* ws = (char*)d_ws;
  float* mod = (float*)(ws + WS_MOD);
  u16* yp = (u16*)(ws + WS_YP);
  u16* x1 = (u16*)(ws + WS_X1);
  u16* h2 = (u16*)(ws + WS_H2);
  u16* hh = (u16*)(ws + WS_HH);
  (void)hipFuncSetAttribute((const void*)k0_conv, hipFuncAttributeMaxDynamicSharedMemorySize, C_LDS);
  (void)hipFuncSetAttribute((const void*)k2_gemm1, hipFuncAttributeMaxDynamicSharedMemorySize, G1_R * G1_SLOT + 256);
  (void)hipFuncSetAttribute((const void*)k3_gemm2, hipFuncAttributeMaxDynamicSharedMemorySize, G2_LDS);
  k0_conv<<<256, 768, C_LDS, stream>>>(kin, vin, shk, ssmk, ssmD, cvec, wmod, mod, yp, x, w1, w3);
  k1_norm<<<256, 512, 0, stream>>>(x, yp, mod, x1, h2, w1, w3, (u16*)(ws + WS_GB), ws + WS_XB);
  k2_gemm1<<<256, 512, G1_R * G1_SLOT + 256, stream>>>(h2, w1, w3, hh, w2);
  k3_gemm2<<<256, 512, G2_LDS, stream>>>(hh, w2, x1, (const u16*)(ws + WS_GB), (u16*)d_out, ws + WS_XB);
}
```
